```python
import torch

def _softmax(x, axis=-1):
    return torch.softmax(x, dim=axis)
def _gelu(x):
    return torch.nn.functional.gelu(x, approximate="tanh")
def _silu(x):
    return torch.nn.functional.silu(x)
def _sigmoid(x):
    return torch.sigmoid(x)
def _layernorm(x, axis=-1, eps=1e-5):
    m = x.mean(dim=axis, keepdim=True)
    v = x.var(dim=axis, keepdim=True, unbiased=False)
    return (x - m) / torch.sqrt(v + eps)
def _scatter_add(tgt, idx, src, axis=0):
    return tgt.clone().index_add_(axis, idx, src)
def _segment_sum(src, seg, n):
    out = torch.zeros((n,) + src.shape[1:], dtype=src.dtype)
    return out.index_add_(0, seg, src)
def _pad(x, pw):
    flat = tuple(v for pair in reversed(pw) for v in pair)
    return torch.nn.functional.pad(x, flat)

kernel_name = 'parallel_sum__linear_kernel_hedgehog__dense_activation_squared_relu__prompt'

def setup_inputs():

    _w = (lambda a: (lambda t: t.to(torch.bfloat16) if t.is_floating_point() else t)(torch.as_tensor(a)))
    def _A_setup():
        B, S, D, H = 1, 2048, 1024, 32
        Dh = D // H
        return {
            "x": _w((lambda *s: torch.randn(*s, dtype=torch.bfloat16) * 0.05)(B, S, D)),
            "wqkv": _w((lambda *s: torch.randn(*s, dtype=torch.bfloat16) * 0.8)(D, 3 * D)),
            "w_phi": _w((lambda *s: torch.randn(*s, dtype=torch.bfloat16) * 0.8)(H, Dh, Dh) * (Dh ** -0.5)),
            "wo": _w((lambda *s: torch.randn(*s, dtype=torch.bfloat16) * 0.2)(D, D)),
        }
    _A_in = _A_setup()

    def _B_setup():
        B, S, D, F = 1, 2048, 1024, 3584
        return {
            "x": _w((lambda *s: torch.randn(*s, dtype=torch.bfloat16) * 0.05)(B, S, D)),
            "w1": _w((lambda *s: torch.randn(*s, dtype=torch.bfloat16) * 0.05)(D, F)),
            "b1": _w((lambda *s: torch.randn(*s, dtype=torch.bfloat16) * 1.6)(F)),
            "w2": _w((lambda *s: torch.randn(*s, dtype=torch.bfloat16) * 0.05)(F, D)),
            "b2": _w((lambda *s: torch.randn(*s, dtype=torch.bfloat16) * 12.8)(D)),
        }
    _B_in = _B_setup()

    return {
        "A_x": _A_in["x"],
        "A_wqkv": _A_in["wqkv"],
        "A_w_phi": _A_in["w_phi"],
        "A_wo": _A_in["wo"],
        "B_x": _B_in["x"],
        "B_w1": _B_in["w1"],
        "B_b1": _B_in["b1"],
        "B_w2": _B_in["w2"],
        "B_b2": _B_in["b2"],
    }

def reference(A_x, A_wqkv, A_w_phi, A_wo, B_x, B_w1, B_b1, B_w2, B_b2):
    def _A_ref(x, wqkv, w_phi, wo):
        B, S, D = x.shape
        H = 32; Dh = D // H
        qkv = (lambda x, *d: x.permute(*d))((x @ wqkv).reshape(B, S, 3, H, Dh), 2, 0, 3, 1, 4)
        q, k, v = qkv[0], qkv[1], qkv[2]
        pq = torch.einsum("bhsd,hdf->bhsf", q, w_phi)
        pk = torch.einsum("bhsd,hdf->bhsf", k, w_phi)
        phi_q = torch.concatenate([_softmax(pq, axis=-1), _softmax(-pq, axis=-1)], axis=-1)
        phi_k = torch.concatenate([_softmax(pk, axis=-1), _softmax(-pk, axis=-1)], axis=-1)
        kv = torch.einsum("bhsf,bhsd->bhfd", phi_k, v)
        z  = torch.einsum("bhsf->bhf", phi_k)
        num = torch.einsum("bhqf,bhfd->bhqd", phi_q, kv)
        den = torch.einsum("bhqf,bhf->bhq", phi_q, z)[..., None] + 1e-6
        o = num / den
        return (lambda x, *d: x.permute(*d))(o, 0, 2, 1, 3).reshape(B, S, D) @ wo

    def _B_ref(x, w1, b1, w2, b2):
        h = torch.einsum("bsd,df->bsf", x, w1) + b1
        zero = torch.zeros_like(h)
        r = torch.where(h > zero, h, zero)
        h = r * r
        out = torch.einsum("bsf,fd->bsd", h, w2) + b2
        return out

    x = A_x
    xn = _layernorm(x, axis=-1)
    return x + _A_ref(xn, A_wqkv, A_w_phi, A_wo) + _B_ref(xn, B_w1, B_b1, B_w2, B_b2)
```

```cpp
#include <hip/hip_runtime.h>
#include <hip/hip_bf16.h>
typedef unsigned short u16;
__device__ inline float b2f(u16 b){ return __uint_as_float(((unsigned)b)<<16); }
__device__ inline u16 f2b(float f){ unsigned u=__float_as_uint(f); u += 0x7fffu + ((u>>16)&1u); return (u16)(u>>16); }
constexpr int S=2048, D=1024, H=32, F=3584;

__global__ void k_ln(const u16* x, float* xn){
  int row=blockIdx.x; int t=threadIdx.x; __shared__ float red[256];
  float v[4]; float s=0; for(int i=0;i<4;i++){ v[i]=b2f(x[row*D+t*4+i]); s+=v[i]; }
  red[t]=s; __syncthreads(); for(int o=128;o>0;o>>=1){ if(t<o) red[t]+=red[t+o]; __syncthreads(); }
  float m=red[0]/D; __syncthreads();
  float q=0; for(int i=0;i<4;i++){ float d=v[i]-m; q+=d*d; }
  red[t]=q; __syncthreads(); for(int o=128;o>0;o>>=1){ if(t<o) red[t]+=red[t+o]; __syncthreads(); }
  float r=rsqrtf(red[0]/D+1e-5f);
  for(int i=0;i<4;i++) xn[row*D+t*4+i]=(v[i]-m)*r;
}
__global__ void k_gemm(const float* A, const u16* B, float* C, int M,int N,int K){
  __shared__ float sa[16][17], sb[16][17];
  int tx=threadIdx.x, ty=threadIdx.y; int r=blockIdx.y*16+ty, c=blockIdx.x*16+tx; float acc=0;
  for(int k=0;k<K;k+=16){ sa[ty][tx]=A[(size_t)r*K+k+tx]; sb[ty][tx]=b2f(B[(size_t)(k+ty)*N+c]); __syncthreads();
    for(int j=0;j<16;j++) acc+=sa[ty][j]*sb[j][tx]; __syncthreads(); }
  C[(size_t)r*N+c]=acc;
}
__global__ void k_phi(const float* qkv, const u16* wphi, float* phi, int which){
  int idx=blockIdx.x*blockDim.x+threadIdx.x; int s=idx/H, h=idx%H;
  float p[32];
  for(int f=0;f<32;f++){ float a=0; for(int d=0;d<32;d++) a+=qkv[(size_t)s*3072+which*1024+h*32+d]*b2f(wphi[h*1024+d*32+f]); p[f]=a; }
  float mx=-1e30f,mn=1e30f; for(int f=0;f<32;f++){mx=fmaxf(mx,p[f]);mn=fminf(mn,p[f]);}
  float sp=0,sn=0; for(int f=0;f<32;f++){sp+=__expf(p[f]-mx); sn+=__expf(mn-p[f]);}
  for(int f=0;f<32;f++){ phi[(size_t)idx*64+f]=__expf(p[f]-mx)/sp; phi[(size_t)idx*64+32+f]=__expf(mn-p[f])/sn; }
}
__global__ void k_kv(const float* phik, const float* qkv, float* kv){
  int h=blockIdx.x, f=blockIdx.y, d=threadIdx.x; float a=0;
  for(int s=0;s<S;s++){ float p=phik[((size_t)s*H+h)*64+f]; float v= d<32? qkv[(size_t)s*3072+2048+h*32+d]:1.f; a+=p*v; }
  if(d<33) kv[(h*64+f)*33+d]=a;
}
__global__ void k_o(const float* phiq, const float* kv, float* o){
  int idx=blockIdx.x*blockDim.x+threadIdx.x; int d=idx%32, h=(idx/32)%H, s=idx/1024;
  float num=0,den=0; for(int f=0;f<64;f++){ float p=phiq[((size_t)s*H+h)*64+f]; num+=p*kv[(h*64+f)*33+d]; den+=p*kv[(h*64+f)*33+32]; }
  o[idx]=num/(den+1e-6f);
}
__global__ void k_act(float* h, const u16* b1){ size_t i=(size_t)blockIdx.x*blockDim.x+threadIdx.x; float v=h[i]+b2f(b1[i%F]); v=v>0?v:0; h[i]=v*v; }
__global__ void k_fin(const u16* x, const float* a, const float* b, const u16* b2, u16* out){ size_t i=(size_t)blockIdx.x*blockDim.x+threadIdx.x; out[i]=f2b(b2f(x[i])+a[i]+b[i]+b2f(b2[i%D])); }

extern "C" void kernel_launch(void* const* d_in, const int* in_sizes, int n_in, void* d_out, int out_size, void* d_ws, size_t ws_size, hipStream_t stream){
  const u16* x=(const u16*)d_in[0]; const u16* wqkv=(const u16*)d_in[1]; const u16* wphi=(const u16*)d_in[2]; const u16* wo=(const u16*)d_in[3];
  const u16* w1=(const u16*)d_in[5]; const u16* b1=(const u16*)d_in[6]; const u16* w2=(const u16*)d_in[7]; const u16* b2=(const u16*)d_in[8];
  float* ws=(float*)d_ws; float* xn=ws; float* qkv=xn+S*D; float* hh=qkv+S*3072; float* phiq=hh+(size_t)S*F; float* phik=phiq+S*2048; float* kv=phik+S*2048; float* o=kv+H*64*33+64; float* oa=o+S*D; float* ob=oa+S*D;
  k_ln<<<S,256,0,stream>>>(x,xn);
  k_gemm<<<dim3(3072/16,S/16),dim3(16,16),0,stream>>>(xn,wqkv,qkv,S,3072,D);
  k_gemm<<<dim3(F/16,S/16),dim3(16,16),0,stream>>>(xn,w1,hh,S,F,D);
  k_phi<<<S*H/64,64,0,stream>>>(qkv,wphi,phiq,0);
  k_phi<<<S*H/64,64,0,stream>>>(qkv,wphi,phik,1);
  k_kv<<<dim3(H,64),64,0,stream>>>(phik,qkv,kv);
  k_o<<<S*D/256,256,0,stream>>>(phiq,kv,o);
  k_act<<<(size_t)S*F/256,256,0,stream>>>(hh,b1);
  k_gemm<<<dim3(D/16,S/16),dim3(16,16),0,stream>>>(o,wo,oa,S,D,D);
  k_gemm<<<dim3(D/16,S/16),dim3(16,16),0,stream>>>(hh,w2,ob,S,D,F);
  k_fin<<<S*D/256,256,0,stream>>>(x,oa,ob,b2,(u16*)d_out);
}
```

```cpp
#include <hip/hip_runtime.h>
#include <utility>

typedef unsigned short u16;
typedef __bf16 bf16x8 __attribute__((ext_vector_type(8)));
typedef __bf16 bf16x2 __attribute__((ext_vector_type(2)));
typedef short s16x4 __attribute__((ext_vector_type(4)));
typedef short s16x8 __attribute__((ext_vector_type(8)));
typedef float f32x2 __attribute__((ext_vector_type(2)));
typedef float f32x4 __attribute__((ext_vector_type(4)));
typedef unsigned u32x2 __attribute__((ext_vector_type(2)));
typedef unsigned u32x4 __attribute__((ext_vector_type(4)));
typedef int i32x4 __attribute__((ext_vector_type(4)));
#define LAS __attribute__((address_space(3)))
#define DI __device__ __forceinline__
#define SB() __builtin_amdgcn_sched_barrier(0)
#define MFMA(a, b, c) __builtin_amdgcn_mfma_f32_16x16x32_bf16((a), (b), (c), 0, 0, 0)

constexpr int S_ = 2048, D_ = 1024, H_ = 32, F_ = 3584;
constexpr int KS2 = 144;
constexpr size_t XN_OFF = 0;
constexpr size_t A2_OFF = 4u << 20;
constexpr size_t A2_BYTES = (size_t)S_ * 4608 * 2;
constexpr size_t PHIQ_OFF = 24u << 20;
constexpr size_t KVP_OFF = 32u << 20;
constexpr int KVP_STRIDE = 2112;

DI float bf2f(u16 b) { return __uint_as_float(((unsigned)b) << 16); }
DI unsigned cvtpk(float lo, float hi) { f32x2 v = {lo, hi}; bf16x2 b = __builtin_convertvector(v, bf16x2); return __builtin_bit_cast(unsigned, b); }
DI float rbf(float x) { return __uint_as_float(cvtpk(x, 0.f) << 16); }
DI float lo_f(unsigned p) { return __uint_as_float(p << 16); }
DI float hi_f(unsigned p) { return __uint_as_float(p & 0xffff0000u); }
DI bf16x8 pack8(const f32x4& a, const f32x4& b) { u32x4 r = {cvtpk(a[0], a[1]), cvtpk(a[2], a[3]), cvtpk(b[0], b[1]), cvtpk(b[2], b[3])}; return __builtin_bit_cast(bf16x8, r); }
DI i32x4 make_srd(const void* p, unsigned bytes) { unsigned long long a = (unsigned long long)p; i32x4 r; r[0] = (int)(unsigned)a; r[1] = (int)((unsigned)(a >> 32) & 0xffffu); r[2] = (int)bytes; r[3] = 0x00020000; return r; }

template <int OFF> DI void dma16(const i32x4& srd, unsigned voff, unsigned soff, unsigned lds) {
  asm volatile("s_mov_b32 m0, %3\n\ts_nop 0\n\tbuffer_load_dwordx4 %0, %1, %2 offen offset:%4 lds" ::"v"(voff), "s"(srd), "s"(soff), "s"(lds), "i"(OFF) : "memory");
}
DI void dma16_masked(const i32x4& srd, unsigned voff, unsigned soff, unsigned lds, unsigned long long mask) {
  asm volatile("s_mov_b32 m0, %3\n\ts_mov_b64 exec, %4\n\tbuffer_load_dwordx4 %0, %1, %2 offen lds\n\ts_mov_b64 exec, -1" ::"v"(voff), "s"(srd), "s"(soff), "s"(lds), "s"(mask) : "memory");
}
template <int N> DI void wait_vm() { asm volatile("s_waitcnt vmcnt(%0)" ::"i"(N) : "memory"); }
DI void bar() { asm volatile("" ::: "memory"); __builtin_amdgcn_s_barrier(); asm volatile("" ::: "memory"); }
DI bf16x8 lds_b128(unsigned addr) { return *(const LAS bf16x8*)(addr); }
DI s16x4 lds_tr(unsigned addr) { return __builtin_amdgcn_ds_read_tr16_b64_v4i16((LAS s16x4*)(addr)); }
DI bf16x8 cat(const s16x4& a, const s16x4& b) { return __builtin_bit_cast(bf16x8, __builtin_shufflevector(a, b, 0, 1, 2, 3, 4, 5, 6, 7)); }

__global__ __launch_bounds__(256) void k_ln(const u16* __restrict__ x, u16* __restrict__ xn) {
  __shared__ float red[2][4][8];
  const int t = threadIdx.x, r = t & 7, c0 = t >> 3, w = t >> 6;
  const int row = blockIdx.x * 8 + r;
  u32x4 v[4];
#pragma unroll
  for (int i = 0; i < 4; ++i) v[i] = *(const u32x4*)(x + (size_t)row * D_ + (c0 + 32 * i) * 8);
  float s = 0.f;
#pragma unroll
  for (int i = 0; i < 4; ++i)
#pragma unroll
    for (int j = 0; j < 4; ++j) s += lo_f(v[i][j]) + hi_f(v[i][j]);
  s += __shfl_xor(s, 8); s += __shfl_xor(s, 16); s += __shfl_xor(s, 32);
  if ((t & 63) < 8) red[0][w][r] = s;
  __syncthreads();
  const float mean = (red[0][0][r] + red[0][1][r] + red[0][2][r] + red[0][3][r]) * (1.f / D_);
  float q = 0.f;
#pragma unroll
  for (int i = 0; i < 4; ++i)
#pragma unroll
    for (int j = 0; j < 4; ++j) { float a = lo_f(v[i][j]) - mean, b = hi_f(v[i][j]) - mean; q += a * a + b * b; }
  q += __shfl_xor(q, 8); q += __shfl_xor(q, 16); q += __shfl_xor(q, 32);
  if ((t & 63) < 8) red[1][w][r] = q;
  __syncthreads();
  const float var = (red[1][0][r] + red[1][1][r] + red[1][2][r] + red[1][3][r]) * (1.f / D_);
  const float mb = rbf(mean);
  const float den = rbf(sqrtf(rbf(rbf(var) + 1e-5f)));
  const float inv = 1.0f / den;
  const int rb = row >> 8, mt = (row >> 4) & 15, rr = row & 15;
#pragma unroll
  for (int i = 0; i < 4; ++i) {
    u32x4 o;
#pragma unroll
    for (int j = 0; j < 4; ++j) {
      unsigned d = cvtpk(lo_f(v[i][j]) - mb, hi_f(v[i][j]) - mb);
      o[j] = cvtpk(lo_f(d) * inv, hi_f(d) * inv);
    }
    const int c = c0 + 32 * i, ks = c >> 2, g = c & 3;
    *(u32x4*)((char*)xn + ((size_t)((rb * 32 + ks) * 16 + mt) * 64 + g * 16 + rr) * 16) = o;
  }
}

constexpr int G1_SLOT = 29696, G1_NSLOT = 5, G1_EXTRA = G1_SLOT * G1_NSLOT, G1_LDS = G1_EXTRA + 4096;
struct G1Dma { i32x4 srd, srdh; unsigned voff, voffh, so0, so1, so2, step, steph; unsigned long long mask; unsigned ldsoff; };

template <int ROLE> DI void g1_dma(G1Dma& d, unsigned slot, int piece);
template <int ROLE, int P> DI void g1_dma_piece(G1Dma& d, unsigned slot) {
  if constexpr (ROLE == 0) {
    if constexpr (P == 0) { dma16<0>(d.srd, d.voff, d.so0, slot + d.ldsoff); d.so0 += d.step; }
    if constexpr (P == 1) { dma16<0>(d.srd, d.voff, d.so1, slot + d.ldsoff + 1024); d.so1 += d.step; }
    if constexpr (P == 2) { dma16<0>(d.srd, d.voff, d.so2, slot + d.ldsoff + 2048); d.so2 += d.step; }
  } else {
    if constexpr (P < 4) { dma16<P * 1024>(d.srd, d.voff, d.so0, slot + d.ldsoff); if constexpr (P == 3) d.so0 += d.step; }
    if constexpr (P == 4) { dma16_masked(d.srdh, d.voffh, d.so1, slot + 28 * 1024, d.mask); d.so1 += d.steph; }
  }
}
template <int ROLE> DI void g1_dma_all(G1Dma& d, unsigned slot) {
  g1_dma_piece<ROLE, 0>(d, slot); g1_dma_piece<ROLE, 1>(d, slot); g1_dma_piece<ROLE, 2>(d, slot);
  if constexpr (ROLE == 1) { g1_dma_piece<ROLE, 3>(d, slot); g1_dma_piece<ROLE, 4>(d, slot); }
}
template <int ROLE> struct G1Frag { bf16x8 a[4]; s16x4 b[ROLE == 0 ? 7 : 6][2]; };
struct G1Addr { unsigned a, l0, l1, h0, h1; };
template <int ROLE, int R> DI void g1_read(G1Frag<ROLE>& f, const G1Addr& ad) {
  if constexpr (R < 2) { f.b[0][R] = lds_tr(ad.l0 + R * 256); }
  else if constexpr (R < 6) { f.a[R - 2] = lds_b128(ad.a + (R - 2) * 1024); }
  else {
    constexpr int t = (R - 4) >> 1, e = (R - 4) & 1;
    if constexpr (t < 6) f.b[t][e] = lds_tr(((t & 1) ? ad.l1 : ad.l0) + (t >> 1) * 2048 + e * 256);
    else f.b[t][e] = lds_tr(e ? ad.h1 : ad.h0);
  }
}
template <int ROLE, int... R> DI void g1_read_all(G1Frag<ROLE>& f, const G1Addr& ad, std::integer_sequence<int, R...>) { (g1_read<ROLE, R>(f, ad), ...); }

template <int ROLE, int I> DI void g1_mfma(f32x4 (&acc)[7][4], const G1Frag<ROLE>& f) {
  constexpr int t = I >> 2, m = I & 3;
  const bf16x8 b = cat(f.b[t][0], f.b[t][1]);
  if constexpr (ROLE == 1 && t >= 4) acc[t][m] = MFMA(f.a[m], b, acc[t][m]);
  else acc[t][m] = MFMA(b, f.a[m], acc[t][m]);
}
template <int ROLE, bool DMA, bool READ, int I> DI void g1_slot(f32x4 (&acc)[7][4], const G1Frag<ROLE>& cur, G1Frag<ROLE>& nxt, const G1Addr& ad, G1Dma& d, unsigned dslot) {
  constexpr int ND = ROLE == 0 ? 3 : 5, NR = ROLE == 0 ? 18 : 16;
  g1_mfma<ROLE, I>(acc, cur);
  if constexpr (READ && I < NR) g1_read<ROLE, I>(nxt, ad);
  if constexpr (DMA && I >= NR && I - NR < ND) g1_dma_piece<ROLE, I - NR>(d, dslot);
  SB();
}
template <int ROLE, bool DMA, bool READ, int... I> DI void g1_body(f32x4 (&acc)[7][4], const G1Frag<ROLE>& cur, G1Frag<ROLE>& nxt, const G1Addr& ad, G1Dma& d, unsigned dslot, std::integer_sequence<int, I...>) {
  (g1_slot<ROLE, DMA, READ, I>(acc, cur, nxt, ad, d, dslot), ...);
}
DI unsigned ring_next(unsigned s) { s += G1_SLOT; return s == G1_SLOT * G1_NSLOT ? 0u : s; }
template <int ROLE, bool DMA, bool READ, int VMSLOTS> DI void g1_step(f32x4 (&acc)[7][4], const G1Frag<ROLE>& cur, G1Frag<ROLE>& nxt, const G1Addr& base, G1Dma& d, unsigned& rslot, unsigned& dslot) {
  constexpr int ND = ROLE == 0 ? 3 : 5, NM = ROLE == 0 ? 28 : 24;
  if constexpr (READ) { wait_vm<VMSLOTS * ND>(); bar(); }
  G1Addr ad = {base.a + rslot, base.l0 + rslot, base.l1 + rslot, base.h0 + rslot, base.h1 + rslot};
  SB();
  g1_body<ROLE, DMA, READ>(acc, cur, nxt, ad, d, dslot, std::make_integer_sequence<int, NM>{});
  rslot = ring_next(rslot); dslot = ring_next(dslot);
}
template <int ROLE> DI void g1_mainloop(f32x4 (&acc)[7][4], const G1Addr& base, G1Dma& d) {
  constexpr int ND = ROLE == 0 ? 3 : 5, NR = ROLE == 0 ? 18 : 16;
  g1_dma_all<ROLE>(d, 0); g1_dma_all<ROLE>(d, G1_SLOT); g1_dma_all<ROLE>(d, 2 * G1_SLOT); g1_dma_all<ROLE>(d, 3 * G1_SLOT);
  G1Frag<ROLE> f0, f1;
  wait_vm<3 * ND>(); bar();
  g1_read_all<ROLE>(f0, base, std::make_integer_sequence<int, NR>{});
  unsigned rslot = G1_SLOT, dslot = 4 * G1_SLOT;
  for (int s = 0; s < 28; s += 2) {
    g1_step<ROLE, true, true, 2>(acc, f0, f1, base, d, rslot, dslot);
    g1_step<ROLE, true, true, 2>(acc, f1, f0, base, d, rslot, dslot);
  }
  g1_step<ROLE, false, true, 2>(acc, f0, f1, base, d, rslot, dslot);
  g1_step<ROLE, false, true, 1>(acc, f1, f0, base, d, rslot, dslot);
  g1_step<ROLE, false, true, 0>(acc, f0, f1, base, d, rslot, dslot);
  g1_step<ROLE, false, false, 0>(acc, f1, f0, base, d, rslot, dslot);
}

DI float xmax(float v) { auto r = __builtin_amdgcn_permlane16_swap(__float_as_uint(v), __float_as_uint(v), false, false); v = fmaxf(__uint_as_float(r[0]), __uint_as_float(r[1]));
  auto q = __builtin_amdgcn_permlane32_swap(__float_as_uint(v), __float_as_uint(v), false, false); return fmaxf(__uint_as_float(q[0]), __uint_as_float(q[1])); }
DI float xsum(float v) { auto r = __builtin_amdgcn_permlane16_swap(__float_as_uint(v), __float_as_uint(v), false, false); v = __uint_as_float(r[0]) + __uint_as_float(r[1]);
  auto q = __builtin_amdgcn_permlane32_swap(__float_as_uint(v), __float_as_uint(v), false, false); return __uint_as_float(q[0]) + __uint_as_float(q[1]); }
DI void dual_softmax(const f32x4& p0, const f32x4& p1, bf16x8& pos, bf16x8& neg) {
  constexpr float L2E = 1.4426950408889634f;
  float p[8];
#pragma unroll
  for (int j = 0; j < 4; j += 2) { unsigned a = cvtpk(p0[j], p0[j + 1]), b = cvtpk(p1[j], p1[j + 1]); p[j] = lo_f(a); p[j + 1] = hi_f(a); p[4 + j] = lo_f(b); p[5 + j] = hi_f(b); }
  float mx = p[0], mn = p[0];
#pragma unroll
  for (int j = 1; j < 8; ++j) { mx = fmaxf(mx, p[j]); mn = fminf(mn, p[j]); }
  mx = xmax(mx); mn = -xmax(-mn);
  float ep[8], en[8], sp = 0.f, sn = 0.f;
#pragma unroll
  for (int j = 0; j < 8; ++j) { ep[j] = __builtin_amdgcn_exp2f((p[j] - mx) * L2E); en[j] = __builtin_amdgcn_exp2f((mn - p[j]) * L2E); sp += ep[j]; sn += en[j]; }
  sp = __builtin_amdgcn_rcpf(xsum(sp)); sn = __builtin_amdgcn_rcpf(xsum(sn));
  u32x4 a, b;
#pragma unroll
  for (int j = 0; j < 4; ++j) { a[j] = cvtpk(ep[2 * j] * sp, ep[2 * j + 1] * sp); b[j] = cvtpk(en[2 * j] * sn, en[2 * j + 1] * sn); }
  pos = __builtin_bit_cast(bf16x8, a); neg = __builtin_bit_cast(bf16x8, b);
}
DI u32x4 frag_natural(unsigned x0, unsigned x1, unsigned y0, unsigned y1) {
  auto r0 = __builtin_amdgcn_permlane16_swap(x0, y0, false, false);
  auto r1 = __builtin_amdgcn_permlane16_swap(x1, y1, false, false);
  u32x4 o = {r0[0], r1[0], r0[1], r1[1]};
  return o;
}

__global__ __launch_bounds__(512, 2) void k_gemm1(const u16* __restrict__ xn, const u16* __restrict__ wqkv, const u16* __restrict__ wphi, const u16* __restrict__ w1,
                                                  const u16* __restrict__ b1, u16* __restrict__ a2, u16* __restrict__ phiq, float* __restrict__ kvp) {
  extern __shared__ __attribute__((aligned(16))) char smem[];
  const int tid = threadIdx.x, wid = __builtin_amdgcn_readfirstlane(tid >> 6), lane = tid & 63, wm = wid & 3;
  const int xcd = blockIdx.x & 7, idx = blockIdx.x >> 3;
  const int rb = (xcd >> 1) * 2 + (idx & 1), hd = (xcd & 1) * 16 + (idx >> 1);
  const int fc0 = 112 * hd, odd = hd & 1;
  const int gcol0 = fc0 + (odd ? 16 : 0), hcol = fc0 + (odd ? 0 : 96);
  const int g = lane >> 4, q = (lane & 15) >> 2, p = lane & 3;
  const unsigned lds0 = (unsigned)(size_t)(LAS char*)smem;
  f32x4 acc[7][4];
#pragma unroll
  for (int t = 0; t < 7; ++t)
#pragma unroll
    for (int m = 0; m < 4; ++m) acc[t][m] = (f32x4){0.f, 0.f, 0.f, 0.f};
  G1Addr base; G1Dma d;
  base.a = lds0 + wm * 4096 + lane * 16;
  const unsigned lb = (g >> 1) * 1024 + (g & 1) * 512 + q * 64 + (g & 1) * 32 + p * 8;
  base.h0 = lds0 + 28 * 1024 + g * 256 + (g & 1) * 128 + q * 32 + p * 8; base.h1 = base.h0 ^ 128;
  if (wid < 4) {
    base.l0 = lds0 + 16384 + 6144 + lb; base.l1 = base.l0 ^ 32;
    const bool isq = wid < 2;
    const unsigned pitch = isq ? 6144u : 7168u;
    d.srd = isq ? make_srd(wqkv, 1024u * 6144u) : make_srd(w1, 1024u * 7168u);
    d.srdh = d.srd; d.voffh = 0; d.steph = 0; d.mask = 0;
    const int r = lane >> 2, c = (lane & 3) ^ (((r >> 3) & 1) << 1);
    d.voff = r * pitch + c * 16;
    unsigned so[3];
#pragma unroll
    for (int i = 0; i < 3; ++i) { const int ub = 3 * wid + i, grp = ub >> 1, u = ub & 1; const int col = isq ? grp * 1024 + hd * 32 : gcol0 + (grp - 3) * 32; so[i] = u * 16 * pitch + col * 2; }
    d.so0 = so[0]; d.so1 = so[1]; d.so2 = so[2]; d.step = 32 * pitch; d.ldsoff = lds0 + 16384 + 3 * wid * 1024;
    if (wid == 0) dma16<0>(make_srd(b1, F_ * 2), lane * 16, fc0 * 2, lds0 + G1_EXTRA + 2048);
    g1_mainloop<0>(acc, base, d);
    bar();
    const int G = ((g & 1) << 1) | (g >> 1);
    const int mtg0 = rb * 16 + wm * 4;
    char* a2b = (char*)a2 + ((size_t)(mtg0 >> 3) * KS2 * 8 + (mtg0 & 7)) * 1024 + (lane & 15) * 16;
    const unsigned bb = lds0 + G1_EXTRA + 2048 + g * 8;
    unsigned hp[7][4][2];
#pragma unroll
    for (int t = 0; t < 7; ++t) {
      const int cof = (t < 6) ? (gcol0 - fc0) + t * 16 : (hcol - fc0);
      const u32x2 bv = *(const LAS u32x2*)(bb + cof * 2);
      const float b0 = lo_f(bv[0]), b1v = hi_f(bv[0]), b2v = lo_f(bv[1]), b3 = hi_f(bv[1]);
#pragma unroll
      for (int m = 0; m < 4; ++m) {
        float v0 = fmaxf(acc[t][m][0] + b0, 0.f), v1 = fmaxf(acc[t][m][1] + b1v, 0.f), v2 = fmaxf(acc[t][m][2] + b2v, 0.f), v3 = fmaxf(acc[t][m][3] + b3, 0.f);
        hp[t][m][0] = cvtpk(v0 * v0, v1 * v1); hp[t][m][1] = cvtpk(v2 * v2, v3 * v3);
      }
    }
#pragma unroll
    for (int u = 0; u < 3; ++u) {
      const int ks = (gcol0 >> 5) + u;
#pragma unroll
      for (int m = 0; m < 4; ++m) *(u32x4*)(a2b + (size_t)(ks * 8 + m) * 1024 + G * 256) = frag_natural(hp[2 * u][m][0], hp[2 * u][m][1], hp[2 * u + 1][m][0], hp[2 * u + 1][m][1]);
    }
    {
      const int ks = hcol >> 5, half = (hcol >> 4) & 1;
#pragma unroll
      for (int m = 0; m < 4; ++m) *(u32x2*)(a2b + (size_t)(ks * 8 + m) * 1024 + (half * 2 + (g >> 1)) * 256 + (g & 1) * 8) = (u32x2){hp[6][m][0], hp[6][m][1]};
    }
  } else {
    base.l0 = lds0 + 16384 + lb; base.l1 = base.l0 ^ 32;
    d.srd = make_srd(xn, S_ * D_ * 2); d.srdh = make_srd(w1, 1024u * 7168u);
    d.voff = lane * 16;
    const int pr = lane >> 1, r = pr ^ (((pr >> 3) & 1) << 2);
    d.voffh = r * 7168 + (lane & 1) * 16;
    d.so0 = (rb * 32 * 16 + 4 * wm) * 1024; d.step = 16384; d.so1 = hcol * 2; d.steph = 32 * 7168; d.so2 = 0;
    d.mask = 0xffffull << (16 * wm); d.ldsoff = lds0 + wm * 4096;
    if (wm < 2) dma16<0>(make_srd(wphi, H_ * 2048), lane * 16, hd * 2048 + wm * 1024, lds0 + G1_EXTRA + wm * 1024);
    g1_mainloop<1>(acc, base, d);
    bar();
    bf16x8 wp[2], idm[2];
#pragma unroll
    for (int ft = 0; ft < 2; ++ft) {
      const unsigned a = lds0 + G1_EXTRA + (4 * g + q) * 64 + ft * 32 + p * 8;
      wp[ft] = cat(lds_tr(a), lds_tr(a + 1024));
      const unsigned one = (g == q) ? ((p & 1) ? 0x3f800000u : 0x00003f80u) : 0u;
      u32x4 iv = {0u, 0u, 0u, 0u};
      iv[ft * 2] = (p >> 1) == 0 ? one : 0u; iv[ft * 2 + 1] = (p >> 1) == 1 ? one : 0u;
      idm[ft] = __builtin_bit_cast(bf16x8, iv);
    }
    const f32x4 zero = {0.f, 0.f, 0.f, 0.f};
    const u32x4 onesu = {0x3f803f80u, 0x3f803f80u, 0x3f803f80u, 0x3f803f80u};
    const bf16x8 ones = __builtin_bit_cast(bf16x8, onesu);
    char* pqb = (char*)phiq + ((size_t)(hd * 128 + rb * 16 + wm * 4) * 2) * 1024 + lane * 16;
#pragma unroll
    for (int m = 0; m < 4; ++m) {
      const bf16x8 qf = pack8(acc[0][m], acc[1][m]);
      bf16x8 pos, neg;
      dual_softmax(MFMA(wp[0], qf, zero), MFMA(wp[1], qf, zero), pos, neg);
      *(bf16x8*)(pqb + (m * 2) * 1024) = pos; *(bf16x8*)(pqb + (m * 2 + 1) * 1024) = neg;
    }
    f32x4 kv[2][4], zz[4];
#pragma unroll
    for (int fq = 0; fq < 4; ++fq) { kv[0][fq] = zero; kv[1][fq] = zero; zz[fq] = zero; }
#pragma unroll
    for (int u = 0; u < 2; ++u) {
      f32x4 tr[2][4];
#pragma unroll
      for (int mm = 0; mm < 2; ++mm) {
        const int m = 2 * u + mm;
        const bf16x8 kf = pack8(acc[2][m], acc[3][m]);
        bf16x8 pos, neg;
        dual_softmax(MFMA(wp[0], kf, zero), MFMA(wp[1], kf, zero), pos, neg);
        tr[mm][0] = MFMA(pos, idm[0], zero); tr[mm][1] = MFMA(pos, idm[1], zero); tr[mm][2] = MFMA(neg, idm[0], zero); tr[mm][3] = MFMA(neg, idm[1], zero);
      }
      const bf16x8 v0 = pack8(acc[4][2 * u], acc[4][2 * u + 1]), v1 = pack8(acc[5][2 * u], acc[5][2 * u + 1]);
#pragma unroll
      for (int fq = 0; fq < 4; ++fq) {
        const bf16x8 ph = pack8(tr[0][fq], tr[1][fq]);
        kv[0][fq] = MFMA(v0, ph, kv[0][fq]); kv[1][fq] = MFMA(v1, ph, kv[1][fq]); zz[fq] = MFMA(ones, ph, zz[fq]);
      }
    }
    LAS float* red = (LAS float*)(lds0 + wm * (KVP_STRIDE * 4));
    const int fl = lane & 15;
#pragma unroll
    for (int fq = 0; fq < 4; ++fq) {
      const int pn = fq >> 1, ft = fq & 1;
#pragma unroll
      for (int t = 0; t < 2; ++t)
#pragma unroll
        for (int r2 = 0; r2 < 4; ++r2) red[((pn * 2 + t) * 64 + (4 * g + r2) + 16 * (fl >> 2)) * 8 + ft * 4 + (fl & 3)] = kv[t][fq][r2];
      if (g == 0) red[2048 + (pn * 4 + (fl >> 2)) * 8 + ft * 4 + (fl & 3)] = zz[fq][0];
    }
    asm volatile("s_waitcnt lgkmcnt(0)" ::: "memory");
    bar();
    const int t2 = tid - 256;
    float* dst = kvp + (size_t)(hd * 8 + rb) * KVP_STRIDE;
#pragma unroll
    for (int i = 0; i < 3; ++i) {
      const int e = (t2 + 256 * i) * 4;
      if (e < KVP_STRIDE) {
        f32x4 s = *(const LAS f32x4*)(lds0 + e * 4);
#pragma unroll
        for (int w = 1; w < 4; ++w) s += *(const LAS f32x4*)(lds0 + w * (KVP_STRIDE * 4) + e * 4);
        *(f32x4*)(dst + e) = s;
      }
    }
  }
}

__global__ __launch_bounds__(256) void k_o(const u16* __restrict__ phiq, const float* __restrict__ kvp, u16* __restrict__ a2) {
  __shared__ __attribute__((aligned(16))) char sm[4096 + 128];
  const int tid = threadIdx.x, lane = tid & 63, wm = tid >> 6, g = lane >> 4;
  const int hd = blockIdx.x >> 3, rb = blockIdx.x & 7;
  const char* pqb = (const char*)phiq + ((size_t)(hd * 128 + rb * 16 + wm * 4) * 2) * 1024 + lane * 16;
  bf16x8 ph[4][2];
#pragma unroll
  for (int m = 0; m < 4; ++m) { ph[m][0] = *(const bf16x8*)(pqb + (m * 2) * 1024); ph[m][1] = *(const bf16x8*)(pqb + (m * 2 + 1) * 1024); }
  const float* src = kvp + (size_t)hd * 8 * KVP_STRIDE;
  f32x4 s0 = {0.f, 0.f, 0.f, 0.f}, s1 = s0;
#pragma unroll
  for (int b = 0; b < 8; ++b) { s0 += *(const f32x4*)(src + b * KVP_STRIDE + tid * 8); s1 += *(const f32x4*)(src + b * KVP_STRIDE + tid * 8 + 4); }
  *(bf16x8*)(sm + tid * 16) = pack8(s0, s1);
  if (tid < 8) {
    f32x4 z0 = {0.f, 0.f, 0.f, 0.f}, z1 = z0;
#pragma unroll
    for (int b = 0; b < 8; ++b) { z0 += *(const f32x4*)(src + b * KVP_STRIDE + 2048 + tid * 8); z1 += *(const f32x4*)(src + b * KVP_STRIDE + 2048 + tid * 8 + 4); }
    *(bf16x8*)(sm + 4096 + tid * 16) = pack8(z0, z1);
  }
  __syncthreads();
  bf16x8 kv[2][2], zf[2];
#pragma unroll
  for (int pn = 0; pn < 2; ++pn) { kv[pn][0] = *(const bf16x8*)(sm + (pn * 2) * 1024 + lane * 16); kv[pn][1] = *(const bf16x8*)(sm + (pn * 2 + 1) * 1024 + lane * 16); zf[pn] = *(const bf16x8*)(sm + 4096 + (pn * 4 + g) * 16); }
  const f32x4 zero = {0.f, 0.f, 0.f, 0.f};
  const int G = ((g & 1) << 1) | (g >> 1);
  const int mtg0 = rb * 16 + wm * 4;
  char* a2b = (char*)a2 + ((size_t)(mtg0 >> 3) * KS2 * 8 + (mtg0 & 7)) * 1024 + (lane & 15) * 16 + (size_t)(112 + hd) * 8 * 1024 + G * 256;
#pragma unroll
  for (int m = 0; m < 4; ++m) {
    f32x4 n0 = MFMA(kv[1][0], ph[m][1], MFMA(kv[0][0], ph[m][0], zero));
    f32x4 n1 = MFMA(kv[1][1], ph[m][1], MFMA(kv[0][1], ph[m][0], zero));
    f32x4 dn = MFMA(zf[1], ph[m][1], MFMA(zf[0], ph[m][0], zero));
    const float inv = 1.0f / (dn[0] + 1e-6f);
    *(u32x4*)(a2b + m * 1024) = frag_natural(cvtpk(n0[0] * inv, n0[1] * inv), cvtpk(n0[2] * inv, n0[3] * inv), cvtpk(n1[0] * inv, n1[1] * inv), cvtpk(n1[2] * inv, n1[3] * inv));
  }
}

constexpr int G2_SLOT = 24576, G2_NSLOT = 6, G2_LDS = G2_SLOT * G2_NSLOT;
struct G2Frag { bf16x8 a[4]; s16x4 b[2][2]; };
struct G2Dma { i32x4 srda, srdb; unsigned voffa, voffb, soa, sob, lda, ldb; };
template <int P> DI void g2_dma_piece(G2Dma& d, unsigned slot) {
  if constexpr (P == 0) dma16<0>(d.srda, d.voffa, d.soa, slot + d.lda);
  if constexpr (P == 1) { dma16<1024>(d.srda, d.voffa, d.soa, slot + d.lda); d.soa += 16384; }
  if constexpr (P == 2) { dma16<0>(d.srdb, d.voffb, d.sob, slot + d.ldb); d.sob += 64 * 2048; }
}
template <int R> DI void g2_read(G2Frag& f, unsigned aa, unsigned l0, unsigned l1) {
  if constexpr (R < 2) f.b[0][R] = lds_tr(l0 + R * 256);
  else if constexpr (R < 6) f.a[R - 2] = lds_b128(aa + (R - 2) * 1024);
  else f.b[1][R - 6] = lds_tr(l1 + (R - 6) * 256);
}
template <bool DMA, bool READ, int I> DI void g2_slot(f32x4 (&acc)[2][4], const G2Frag& cur, G2Frag& nxt, unsigned aa, unsigned l0, unsigned l1, G2Dma& d, unsigned dslot) {
  constexpr int t = I >> 2, m = I & 3;
  acc[t][m] = MFMA(cat(cur.b[t][0], cur.b[t][1]), cur.a[m], acc[t][m]);
  if constexpr (READ) g2_read<I>(nxt, aa, l0, l1);
  if constexpr (DMA && I >= 5) g2_dma_piece<I - 5>(d, dslot);
  SB();
}
template <bool DMA, bool READ, int... I> DI void g2_body(f32x4 (&acc)[2][4], const G2Frag& cur, G2Frag& nxt, unsigned aa, unsigned l0, unsigned l1, G2Dma& d, unsigned dslot, std::integer_sequence<int, I...>) {
  (g2_slot<DMA, READ, I>(acc, cur, nxt, aa, l0, l1, d, dslot), ...);
}
DI unsigned ring2_next(unsigned s) { s += G2_SLOT; return s == G2_SLOT * G2_NSLOT ? 0u : s; }
template <bool DMA, bool READ, int VMSLOTS> DI void g2_step(f32x4 (&acc)[2][4], const G2Frag& cur, G2Frag& nxt, unsigned aa, unsigned l0, G2Dma& d, unsigned& rslot, unsigned& dslot) {
  if constexpr (READ) { wait_vm<VMSLOTS * 3>(); bar(); }
  const unsigned a = aa + rslot, b0 = l0 + rslot, b1 = (l0 ^ 32) + rslot;
  SB();
  g2_body<DMA, READ>(acc, cur, nxt, a, b0, b1, d, dslot, std::make_integer_sequence<int, 8>{});
  rslot = ring2_next(rslot); dslot = ring2_next(dslot);
}
__global__ __launch_bounds__(512, 2) void k_gemm2(const u16* __restrict__ a2, const u16* __restrict__ w2, const u16* __restrict__ wo, const u16* __restrict__ x,
                                                  const u16* __restrict__ b2, u16* __restrict__ out) {
  extern __shared__ __attribute__((aligned(16))) char smem[];
  const int tid = threadIdx.x, wid = __builtin_amdgcn_readfirstlane(tid >> 6), lane = tid & 63;
  const int kg = wid >> 2, wm = (wid >> 1) & 1, wn = wid & 1;
  const int xcd = blockIdx.x & 7, idx = blockIdx.x >> 3;
  const int rt = (xcd >> 1) * 4 + (idx & 3), ct = (xcd & 1) * 8 + (idx >> 2);
  const int g = lane >> 4, q = (lane & 15) >> 2, p = lane & 3;
  const unsigned lds0 = (unsigned)(size_t)(LAS char*)smem;
  const unsigned aa = lds0 + kg * 8192 + wm * 4096 + lane * 16;
  const unsigned l0 = lds0 + 16384 + kg * 4096 + wn * 2048 + (g >> 1) * 1024 + (g & 1) * 512 + q * 64 + (g & 1) * 32 + p * 8;
  G2Dma d;
  d.srda = make_srd(a2, (unsigned)A2_BYTES); d.srdb = make_srd(w2, F_ * 2048u);
  d.voffa = lane * 16; d.soa = (rt * KS2 * 8 + wid * 2) * 1024; d.lda = lds0 + wid * 2048;
  { const int r = lane >> 2, c = (lane & 3) ^ (((r >> 3) & 1) << 1); d.voffb = r * 2048 + c * 16; }
  const unsigned sob0 = ((wid >> 2) * 32 + (wid & 1) * 16) * 2048 + (ct * 64 + ((wid >> 1) & 1) * 32) * 2;
  d.sob = sob0; d.ldb = lds0 + 16384 + wid * 1024;
  f32x4 acc[2][4];
#pragma unroll
  for (int t = 0; t < 2; ++t)
#pragma unroll
    for (int m = 0; m < 4; ++m) acc[t][m] = (f32x4){0.f, 0.f, 0.f, 0.f};
#pragma unroll
  for (int s = 0; s < 5; ++s) { g2_dma_piece<0>(d, s * G2_SLOT); g2_dma_piece<1>(d, s * G2_SLOT); g2_dma_piece<2>(d, s * G2_SLOT); }
  G2Frag f0, f1;
  wait_vm<12>(); bar();
  g2_read<0>(f0, aa, l0, l0 ^ 32); g2_read<1>(f0, aa, l0, l0 ^ 32); g2_read<2>(f0, aa, l0, l0 ^ 32); g2_read<3>(f0, aa, l0, l0 ^ 32);
  g2_read<4>(f0, aa, l0, l0 ^ 32); g2_read<5>(f0, aa, l0, l0 ^ 32); g2_read<6>(f0, aa, l0, l0 ^ 32); g2_read<7>(f0, aa, l0, l0 ^ 32);
  unsigned rslot = G2_SLOT, dslot = 5 * G2_SLOT;
  for (int s = 0; s < 66; s += 2) {
    g2_step<true, true, 3>(acc, f0, f1, aa, l0, d, rslot, dslot);
    if (s == 50) { d.srdb = make_srd(wo, 1024u * 2048u); d.sob = sob0; }
    g2_step<true, true, 3>(acc, f1, f0, aa, l0, d, rslot, dslot);
  }
  g2_step<true, true, 3>(acc, f0, f1, aa, l0, d, rslot, dslot);
  g2_step<false, true, 3>(acc, f1, f0, aa, l0, d, rslot, dslot);
  g2_step<false, true, 2>(acc, f0, f1, aa, l0, d, rslot, dslot);
  g2_step<false, true, 1>(acc, f1, f0, aa, l0, d, rslot, dslot);
  g2_step<false, true, 0>(acc, f0, f1, aa, l0, d, rslot, dslot);
  g2_step<false, false, 0>(acc, f1, f0, aa, l0, d, rslot, dslot);
  bar();
  constexpr int PITCH = 68 * 4;
#pragma unroll
  for (int t = 0; t < 2; ++t)
#pragma unroll
    for (int m = 0; m < 4; ++m) *(LAS f32x4*)(lds0 + (kg * 128 + wm * 64 + m * 16 + (lane & 15)) * PITCH + (wn * 32 + t * 16 + g * 4) * 4) = acc[t][m];
  asm volatile("s_waitcnt lgkmcnt(0)" ::: "memory");
  bar();
#pragma unroll
  for (int i = 0; i < 2; ++i) {
    const int task = tid + 512 * i, row = task >> 3, c8 = (task & 7) * 8;
    const unsigned la = lds0 + row * PITCH + c8 * 4;
    f32x4 v0 = *(const LAS f32x4*)(la) + *(const LAS f32x4*)(la + 128 * PITCH);
    f32x4 v1 = *(const LAS f32x4*)(la + 16) + *(const LAS f32x4*)(la + 128 * PITCH + 16);
    const size_t go = (size_t)(rt * 128 + row) * D_ + ct * 64 + c8;
    const u32x4 xv = *(const u32x4*)(x + go), bv = *(const u32x4*)(b2 + ct * 64 + c8);
    u32x4 o;
    o[0] = cvtpk(v0[0] + lo_f(xv[0]) + lo_f(bv[0]), v0[1] + hi_f(xv[0]) + hi_f(bv[0]));
    o[1] = cvtpk(v0[2] + lo_f(xv[1]) + lo_f(bv[1]), v0[3] + hi_f(xv[1]) + hi_f(bv[1]));
    o[2] = cvtpk(v1[0] + lo_f(xv[2]) + lo_f(bv[2]), v1[1] + hi_f(xv[2]) + hi_f(bv[2]));
    o[3] = cvtpk(v1[2] + lo_f(xv[3]) + lo_f(bv[3]), v1[3] + hi_f(xv[3]) + hi_f(bv[3]));
    *(u32x4*)(out + go) = o;
  }
}

extern "C" void kernel_launch(void* const* d_in, const int* in_sizes, int n_in, void* d_out, int out_size, void* d_ws, size_t ws_size, hipStream_t stream) {
  const u16* x = (const u16*)d_in[0]; const u16* wqkv = (const u16*)d_in[1]; const u16* wphi = (const u16*)d_in[2]; const u16* wo = (const u16*)d_in[3];
  const u16* w1 = (const u16*)d_in[5]; const u16* b1 = (const u16*)d_in[6]; const u16* w2 = (const u16*)d_in[7]; const u16* b2 = (const u16*)d_in[8];
  char* ws = (char*)d_ws;
  u16* xn = (u16*)(ws + XN_OFF); u16* a2 = (u16*)(ws + A2_OFF); u16* phiq = (u16*)(ws + PHIQ_OFF); float* kvp = (float*)(ws + KVP_OFF);
  hipFuncSetAttribute((const void*)k_gemm1, hipFuncAttributeMaxDynamicSharedMemorySize, G1_LDS);
  hipFuncSetAttribute((const void*)k_gemm2, hipFuncAttributeMaxDynamicSharedMemorySize, G2_LDS);
  k_ln<<<256, 256, 0, stream>>>(x, xn);
  k_gemm1<<<256, 512, G1_LDS, stream>>>(xn, wqkv, wphi, w1, b1, a2, phiq, kvp);
  k_o<<<256, 256, 0, stream>>>(phiq, kvp, a2);
  k_gemm2<<<256, 512, G2_LDS, stream>>>(a2, w2, wo, x, b2, (u16*)d_out);
}
```

```cpp
#define PREFETCH 2
#include <hip/hip_runtime.h>
#include <utility>

typedef unsigned short u16;
typedef __bf16 bf16x8 __attribute__((ext_vector_type(8)));
typedef __bf16 bf16x2 __attribute__((ext_vector_type(2)));
typedef short s16x4 __attribute__((ext_vector_type(4)));
typedef short s16x8 __attribute__((ext_vector_type(8)));
typedef float f32x2 __attribute__((ext_vector_type(2)));
typedef float f32x4 __attribute__((ext_vector_type(4)));
typedef unsigned u32x2 __attribute__((ext_vector_type(2)));
typedef unsigned u32x4 __attribute__((ext_vector_type(4)));
typedef int i32x4 __attribute__((ext_vector_type(4)));
#define LAS __attribute__((address_space(3)))
#define DI __device__ __forceinline__
#define SB() __builtin_amdgcn_sched_barrier(0)
#define MFMA(a, b, c) __builtin_amdgcn_mfma_f32_16x16x32_bf16((a), (b), (c), 0, 0, 0)

constexpr int S_ = 2048, D_ = 1024, H_ = 32, F_ = 3584;
constexpr int KS2 = 144;
constexpr size_t XN_OFF = 0;
constexpr size_t A2_OFF = 4u << 20;
constexpr size_t A2_BYTES = (size_t)S_ * 4608 * 2;
constexpr size_t PHIQ_OFF = 24u << 20;
constexpr size_t KVP_OFF = 32u << 20;
constexpr int KVP_STRIDE = 2112;

DI float bf2f(u16 b) { return __uint_as_float(((unsigned)b) << 16); }
DI unsigned cvtpk(float lo, float hi) { f32x2 v = {lo, hi}; bf16x2 b = __builtin_convertvector(v, bf16x2); return __builtin_bit_cast(unsigned, b); }
DI float rbf(float x) { return __uint_as_float(cvtpk(x, 0.f) << 16); }
DI float lo_f(unsigned p) { return __uint_as_float(p << 16); }
DI float hi_f(unsigned p) { return __uint_as_float(p & 0xffff0000u); }
DI bf16x8 pack8(const f32x4& a, const f32x4& b) { u32x4 r = {cvtpk(a[0], a[1]), cvtpk(a[2], a[3]), cvtpk(b[0], b[1]), cvtpk(b[2], b[3])}; return __builtin_bit_cast(bf16x8, r); }
DI i32x4 make_srd(const void* p, unsigned bytes) { unsigned long long a = (unsigned long long)p; i32x4 r; r[0] = (int)(unsigned)a; r[1] = (int)((unsigned)(a >> 32) & 0xffffu); r[2] = (int)bytes; r[3] = 0x00020000; return r; }

#define WNTS ""
template <int OFF> DI void dma16(const i32x4& srd, unsigned voff, unsigned soff, unsigned lds) {
  asm volatile("s_mov_b32 m0, %3\n\ts_nop 0\n\tbuffer_load_dwordx4 %0, %1, %2 offen offset:%4 lds" ::"v"(voff), "s"(srd), "s"(soff), "s"(lds), "i"(OFF) : "memory");
}
template <int OFF> DI void dma16w(const i32x4& srd, unsigned voff, unsigned soff, unsigned lds) {
  asm volatile("s_mov_b32 m0, %3\n\ts_nop 0\n\tbuffer_load_dwordx4 %0, %1, %2 offen offset:%4" WNTS " lds" ::"v"(voff), "s"(srd), "s"(soff), "s"(lds), "i"(OFF) : "memory");
}
DI void dma16_masked(const i32x4& srd, unsigned voff, unsigned soff, unsigned lds, unsigned long long mask) {
  asm volatile("s_mov_b32 m0, %3\n\ts_mov_b64 exec, %4\n\tbuffer_load_dwordx4 %0, %1, %2 offen" WNTS " lds\n\ts_mov_b64 exec, -1" ::"v"(voff), "s"(srd), "s"(soff), "s"(lds), "s"(mask) : "memory");
}
template <int N> DI void wait_vm() { asm volatile("s_waitcnt vmcnt(%0)" ::"i"(N) : "memory"); }
DI void bar() { asm volatile("" ::: "memory"); __builtin_amdgcn_s_barrier(); asm volatile("" ::: "memory"); }
DI bf16x8 lds_b128(unsigned addr) { return *(const LAS bf16x8*)(addr); }
DI s16x4 lds_tr(unsigned addr) { return __builtin_amdgcn_ds_read_tr16_b64_v4i16((LAS s16x4*)(addr)); }
DI bf16x8 cat(const s16x4& a, const s16x4& b) { return __builtin_bit_cast(bf16x8, __builtin_shufflevector(a, b, 0, 1, 2, 3, 4, 5, 6, 7)); }

#define ST_ARG
#define ST_PASS
#define ST_INIT(ws, k)
#define ST_MARK(i)
#define ST_FIN(i)
#define XLOAD(p) (*(p))
__global__ __launch_bounds__(256) void k_ln(const u16* __restrict__ x, u16* __restrict__ xn, const u16* wqkv, const u16* w1, const u16* w2, const u16* wo) {
  __shared__ float red[2][4][8];
  const int t = threadIdx.x, r = t & 7, c0 = t >> 3, w = t >> 6;
  ST_INIT(xn, 0);
  const int row = blockIdx.x * 8 + r;
  u32x4 v[4];
#pragma unroll
  for (int i = 0; i < 4; ++i) v[i] = XLOAD((const u32x4*)(x + (size_t)row * D_ + (c0 + 32 * i) * 8));
  unsigned pf0 = 0, pf1 = 0, pf2 = 0, pf3 = 0;
  { const unsigned gt = blockIdx.x * 256 + t;
    if (gt < 49152) asm volatile("global_load_dword %0, %1, %2" : "=v"(pf0) : "v"(gt * 128), "s"(wqkv));
    if (gt < 57344) asm volatile("global_load_dword %0, %1, %2" : "=v"(pf1) : "v"(gt * 128), "s"(w1));
    if (gt < 57344) asm volatile("global_load_dword %0, %1, %2" : "=v"(pf2) : "v"(gt * 128), "s"(w2));
    if (gt < 16384) asm volatile("global_load_dword %0, %1, %2" : "=v"(pf3) : "v"(gt * 128), "s"(wo));
  }
  float s = 0.f;
#pragma unroll
  for (int i = 0; i < 4; ++i)
#pragma unroll
    for (int j = 0; j < 4; ++j) s += lo_f(v[i][j]) + hi_f(v[i][j]);
  s += __shfl_xor(s, 8); s += __shfl_xor(s, 16); s += __shfl_xor(s, 32);
  if ((t & 63) < 8) red[0][w][r] = s;
  __syncthreads();
  const float mean = (red[0][0][r] + red[0][1][r] + red[0][2][r] + red[0][3][r]) * (1.f / D_);
  float q = 0.f;
#pragma unroll
  for (int i = 0; i < 4; ++i)
#pragma unroll
    for (int j = 0; j < 4; ++j) { float a = lo_f(v[i][j]) - mean, b = hi_f(v[i][j]) - mean; q += a * a + b * b; }
  q += __shfl_xor(q, 8); q += __shfl_xor(q, 16); q += __shfl_xor(q, 32);
  if ((t & 63) < 8) red[1][w][r] = q;
  __syncthreads();
  const float var = (red[1][0][r] + red[1][1][r] + red[1][2][r] + red[1][3][r]) * (1.f / D_);
  const float mb = rbf(mean);
  const float den = rbf(sqrtf(rbf(rbf(var) + 1e-5f)));
  const float inv = 1.0f / den;
  const int rb = row >> 8, mt = (row >> 4) & 15, rr = row & 15;
#pragma unroll
  for (int i = 0; i < 4; ++i) {
    u32x4 o;
#pragma unroll
    for (int j = 0; j < 4; ++j) {
      unsigned d = cvtpk(lo_f(v[i][j]) - mb, hi_f(v[i][j]) - mb);
      o[j] = cvtpk(lo_f(d) * inv, hi_f(d) * inv);
    }
    const int c = c0 + 32 * i, ks = c >> 2, g = c & 3;
    *(u32x4*)((char*)xn + ((size_t)((rb * 32 + ks) * 16 + mt) * 64 + g * 16 + rr) * 16) = o;
  }
  ST_MARK(1); wait_vm<0>(); ST_FIN(2);
  asm volatile("s_waitcnt vmcnt(0)" : "+v"(pf0), "+v"(pf1), "+v"(pf2), "+v"(pf3));
}

#define G1_DMAPOS 0
constexpr int G1_SLOT = 29696, G1_NSLOT = 5, G1_EXTRA = G1_SLOT * G1_NSLOT, G1_LDS = G1_EXTRA + 4096;
struct G1Dma { i32x4 srd, srdh; unsigned voff, voffh, so0, so1, so2, step, steph; unsigned long long mask; unsigned ldsoff; };

template <int ROLE> DI void g1_dma(G1Dma& d, unsigned slot, int piece);
template <int ROLE, int P> DI void g1_dma_piece(G1Dma& d, unsigned slot) {
  if constexpr (ROLE == 0) {
    if constexpr (P == 0) { dma16w<0>(d.srd, d.voff, d.so0, slot + d.ldsoff); d.so0 += d.step; }
    if constexpr (P == 1) { dma16w<0>(d.srd, d.voff, d.so1, slot + d.ldsoff + 1024); d.so1 += d.step; }
    if constexpr (P == 2) { dma16w<0>(d.srd, d.voff, d.so2, slot + d.ldsoff + 2048); d.so2 += d.step; }
  } else {
    if constexpr (P < 4) { dma16<P * 1024>(d.srd, d.voff, d.so0, slot + d.ldsoff); if constexpr (P == 3) d.so0 += d.step; }
    if constexpr (P == 4) { dma16_masked(d.srdh, d.voffh, d.so1, slot + 28 * 1024, d.mask); d.so1 += d.steph; }
  }
}
template <int ROLE> DI void g1_dma_all(G1Dma& d, unsigned slot) {
  g1_dma_piece<ROLE, 0>(d, slot); g1_dma_piece<ROLE, 1>(d, slot); g1_dma_piece<ROLE, 2>(d, slot);
  if constexpr (ROLE == 1) { g1_dma_piece<ROLE, 3>(d, slot); g1_dma_piece<ROLE, 4>(d, slot); }
}
template <int ROLE> struct G1Frag { bf16x8 a[4]; s16x4 b[ROLE == 0 ? 7 : 6][2]; };
struct G1Addr { unsigned a, l0, l1, h0, h1; };
template <int ROLE, int R> DI void g1_read(G1Frag<ROLE>& f, const G1Addr& ad) {
  if constexpr (R < 2) { f.b[0][R] = lds_tr(ad.l0 + R * 256); }
  else if constexpr (R < 6) { f.a[R - 2] = lds_b128(ad.a + (R - 2) * 1024); }
  else {
    constexpr int t = (R - 4) >> 1, e = (R - 4) & 1;
    if constexpr (t < 6) f.b[t][e] = lds_tr(((t & 1) ? ad.l1 : ad.l0) + (t >> 1) * 2048 + e * 256);
    else f.b[t][e] = lds_tr(e ? ad.h1 : ad.h0);
  }
}
template <int ROLE, int... R> DI void g1_read_all(G1Frag<ROLE>& f, const G1Addr& ad, std::integer_sequence<int, R...>) { (g1_read<ROLE, R>(f, ad), ...); }

template <int ROLE, int I> DI void g1_mfma(f32x4 (&acc)[7][4], const G1Frag<ROLE>& f) {
  constexpr int t = I >> 2, m = I & 3;
  const bf16x8 b = cat(f.b[t][0], f.b[t][1]);
  if constexpr (ROLE == 1 && t >= 4) acc[t][m] = MFMA(f.a[m], b, acc[t][m]);
  else acc[t][m] = MFMA(b, f.a[m], acc[t][m]);
}
template <int ROLE, bool DMA, bool READ, int I> DI void g1_slot(f32x4 (&acc)[7][4], const G1Frag<ROLE>& cur, G1Frag<ROLE>& nxt, const G1Addr& ad, G1Dma& d, unsigned dslot) {
  constexpr int ND = ROLE == 0 ? 3 : 5, NR = ROLE == 0 ? 18 : 16;
  g1_mfma<ROLE, I>(acc, cur);
  if constexpr (READ && I < NR) g1_read<ROLE, I>(nxt, ad);
  if constexpr (DMA && I >= NR && I - NR < ND) g1_dma_piece<ROLE, I - NR>(d, dslot);
  SB();
}
template <int ROLE, bool DMA, bool READ, int... I> DI void g1_body(f32x4 (&acc)[7][4], const G1Frag<ROLE>& cur, G1Frag<ROLE>& nxt, const G1Addr& ad, G1Dma& d, unsigned dslot, std::integer_sequence<int, I...>) {
  (g1_slot<ROLE, DMA, READ, I>(acc, cur, nxt, ad, d, dslot), ...);
}
DI unsigned ring_next(unsigned s) { s += G1_SLOT; return s == G1_SLOT * G1_NSLOT ? 0u : s; }
template <int ROLE, bool DMA, bool READ, int VMSLOTS> DI void g1_step(f32x4 (&acc)[7][4], const G1Frag<ROLE>& cur, G1Frag<ROLE>& nxt, const G1Addr& base, G1Dma& d, unsigned& rslot, unsigned& dslot) {
  constexpr int ND = ROLE == 0 ? 3 : 5, NM = ROLE == 0 ? 28 : 24;
  if constexpr (READ) { wait_vm<VMSLOTS * ND>(); bar(); }
  G1Addr ad = {base.a + rslot, base.l0 + rslot, base.l1 + rslot, base.h0 + rslot, base.h1 + rslot};
  SB();
  g1_body<ROLE, DMA, READ>(acc, cur, nxt, ad, d, dslot, std::make_integer_sequence<int, NM>{});
  rslot = ring_next(rslot); dslot = ring_next(dslot);
}
template <int ROLE> DI void g1_mainloop(f32x4 (&acc)[7][4], const G1Addr& base, G1Dma& d ST_ARG) {
  constexpr int ND = ROLE == 0 ? 3 : 5, NR = ROLE == 0 ? 18 : 16;
  g1_dma_all<ROLE>(d, 0); g1_dma_all<ROLE>(d, G1_SLOT); g1_dma_all<ROLE>(d, 2 * G1_SLOT); g1_dma_all<ROLE>(d, 3 * G1_SLOT);
  G1Frag<ROLE> f0, f1;
  wait_vm<3 * ND>(); bar();
  ST_MARK(1);
  g1_read_all<ROLE>(f0, base, std::make_integer_sequence<int, NR>{});
  unsigned rslot = G1_SLOT, dslot = 4 * G1_SLOT;
  for (int s = 0; s < 28; s += 2) {
    g1_step<ROLE, true, true, 2>(acc, f0, f1, base, d, rslot, dslot);
    g1_step<ROLE, true, true, 2>(acc, f1, f0, base, d, rslot, dslot);
  }
  g1_step<ROLE, false, true, 2>(acc, f0, f1, base, d, rslot, dslot);
  g1_step<ROLE, false, true, 1>(acc, f1, f0, base, d, rslot, dslot);
  g1_step<ROLE, false, true, 0>(acc, f0, f1, base, d, rslot, dslot);
  g1_step<ROLE, false, false, 0>(acc, f1, f0, base, d, rslot, dslot);
}

DI float xmax(float v) { auto r = __builtin_amdgcn_permlane16_swap(__float_as_uint(v), __float_as_uint(v), false, false); v = fmaxf(__uint_as_float(r[0]), __uint_as_float(r[1]));
  auto q = __builtin_amdgcn_permlane32_swap(__float_as_uint(v), __float_as_uint(v), false, false); return fmaxf(__uint_as_float(q[0]), __uint_as_float(q[1])); }
DI float xsum(float v) { auto r = __builtin_amdgcn_permlane16_swap(__float_as_uint(v), __float_as_uint(v), false, false); v = __uint_as_float(r[0]) + __uint_as_float(r[1]);
  auto q = __builtin_amdgcn_permlane32_swap(__float_as_uint(v), __float_as_uint(v), false, false); return __uint_as_float(q[0]) + __uint_as_float(q[1]); }
DI void dual_softmax(const f32x4& p0, const f32x4& p1, bf16x8& pos, bf16x8& neg) {
  constexpr float L2E = 1.4426950408889634f;
  float p[8];
#pragma unroll
  for (int j = 0; j < 4; j += 2) { unsigned a = cvtpk(p0[j], p0[j + 1]), b = cvtpk(p1[j], p1[j + 1]); p[j] = lo_f(a); p[j + 1] = hi_f(a); p[4 + j] = lo_f(b); p[5 + j] = hi_f(b); }
  float mx = p[0], mn = p[0];
#pragma unroll
  for (int j = 1; j < 8; ++j) { mx = fmaxf(mx, p[j]); mn = fminf(mn, p[j]); }
  mx = xmax(mx); mn = -xmax(-mn);
  float ep[8], en[8], sp = 0.f, sn = 0.f;
#pragma unroll
  for (int j = 0; j < 8; ++j) { ep[j] = __builtin_amdgcn_exp2f((p[j] - mx) * L2E); en[j] = __builtin_amdgcn_exp2f((mn - p[j]) * L2E); sp += ep[j]; sn += en[j]; }
  sp = __builtin_amdgcn_rcpf(xsum(sp)); sn = __builtin_amdgcn_rcpf(xsum(sn));
  u32x4 a, b;
#pragma unroll
  for (int j = 0; j < 4; ++j) { a[j] = cvtpk(ep[2 * j] * sp, ep[2 * j + 1] * sp); b[j] = cvtpk(en[2 * j] * sn, en[2 * j + 1] * sn); }
  pos = __builtin_bit_cast(bf16x8, a); neg = __builtin_bit_cast(bf16x8, b);
}
DI u32x4 frag_natural(unsigned x0, unsigned x1, unsigned y0, unsigned y1) {
  auto r0 = __builtin_amdgcn_permlane16_swap(x0, y0, false, false);
  auto r1 = __builtin_amdgcn_permlane16_swap(x1, y1, false, false);
  u32x4 o = {r0[0], r1[0], r0[1], r1[1]};
  return o;
}

__global__ __launch_bounds__(512, 2) void k_gemm1(const u16* __restrict__ xn, const u16* __restrict__ wqkv, const u16* __restrict__ wphi, const u16* __restrict__ w1,
                                                  const u16* __restrict__ b1, u16* __restrict__ a2, u16* __restrict__ phiq, float* __restrict__ kvp) {
  extern __shared__ __attribute__((aligned(16))) char smem[];
  const int tid = threadIdx.x, wid = __builtin_amdgcn_readfirstlane(tid >> 6), lane = tid & 63, wm = wid & 3;
  ST_INIT(xn, 1);
  const int xcd = blockIdx.x & 7, idx = blockIdx.x >> 3;
  const int rb = (xcd >> 1) * 2 + (idx & 1), hd = (xcd & 1) * 16 + (idx >> 1);
  const int fc0 = 112 * hd, odd = hd & 1;
  const int gcol0 = fc0 + (odd ? 16 : 0), hcol = fc0 + (odd ? 0 : 96);
  const int g = lane >> 4, q = (lane & 15) >> 2, p = lane & 3;
  const unsigned lds0 = (unsigned)(size_t)(LAS char*)smem;
  f32x4 acc[7][4];
#pragma unroll
  for (int t = 0; t < 7; ++t)
#pragma unroll
    for (int m = 0; m < 4; ++m) acc[t][m] = (f32x4){0.f, 0.f, 0.f, 0.f};
  G1Addr base; G1Dma d;
  base.a = lds0 + wm * 4096 + lane * 16;
  const unsigned lb = (g >> 1) * 1024 + (g & 1) * 512 + q * 64 + (g & 1) * 32 + p * 8;
  base.h0 = lds0 + 28 * 1024 + g * 256 + (g & 1) * 128 + q * 32 + p * 8; base.h1 = base.h0 ^ 128;
  if (wid < 4) {
    base.l0 = lds0 + 16384 + 6144 + lb; base.l1 = base.l0 ^ 32;
    const bool isq = wid < 2;
    const unsigned pitch = isq ? 6144u : 7168u;
    d.srd = isq ? make_srd(wqkv, 1024u * 6144u) : make_srd(w1, 1024u * 7168u);
    d.srdh = d.srd; d.voffh = 0; d.steph = 0; d.mask = 0;
    const int r = lane >> 2, c = (lane & 3) ^ (((r >> 3) & 1) << 1);
    d.voff = r * pitch + c * 16;
    unsigned so[3];
#pragma unroll
    for (int i = 0; i < 3; ++i) { const int ub = 3 * wid + i, grp = ub >> 1, u = ub & 1; const int col = isq ? grp * 1024 + hd * 32 : gcol0 + (grp - 3) * 32; so[i] = u * 16 * pitch + col * 2; }
    d.so0 = so[0]; d.so1 = so[1]; d.so2 = so[2]; d.step = 32 * pitch; d.ldsoff = lds0 + 16384 + 3 * wid * 1024;
    if (wid == 0) dma16<0>(make_srd(b1, F_ * 2), lane * 16, fc0 * 2, lds0 + G1_EXTRA + 2048);
    g1_mainloop<0>(acc, base, d ST_PASS);
    ST_MARK(2);
    bar();
    const int G = ((g & 1) << 1) | (g >> 1);
    const int mtg0 = rb * 16 + wm * 4;
    char* a2b = (char*)a2 + ((size_t)(mtg0 >> 3) * KS2 * 8 + (mtg0 & 7)) * 1024 + (lane & 15) * 16;
    const unsigned bb = lds0 + G1_EXTRA + 2048 + g * 8;
    unsigned hp[7][4][2];
#pragma unroll
    for (int t = 0; t < 7; ++t) {
      const int cof = (t < 6) ? (gcol0 - fc0) + t * 16 : (hcol - fc0);
      const u32x2 bv = *(const LAS u32x2*)(bb + cof * 2);
      const float b0 = lo_f(bv[0]), b1v = hi_f(bv[0]), b2v = lo_f(bv[1]), b3 = hi_f(bv[1]);
#pragma unroll
      for (int m = 0; m < 4; ++m) {
        float v0 = fmaxf(acc[t][m][0] + b0, 0.f), v1 = fmaxf(acc[t][m][1] + b1v, 0.f), v2 = fmaxf(acc[t][m][2] + b2v, 0.f), v3 = fmaxf(acc[t][m][3] + b3, 0.f);
        hp[t][m][0] = cvtpk(v0 * v0, v1 * v1); hp[t][m][1] = cvtpk(v2 * v2, v3 * v3);
      }
    }
#pragma unroll
    for (int u = 0; u < 3; ++u) {
      const int ks = (gcol0 >> 5) + u;
#pragma unroll
      for (int m = 0; m < 4; ++m) *(u32x4*)(a2b + (size_t)(ks * 8 + m) * 1024 + G * 256) = frag_natural(hp[2 * u][m][0], hp[2 * u][m][1], hp[2 * u + 1][m][0], hp[2 * u + 1][m][1]);
    }
    {
      const int ks = hcol >> 5, half = (hcol >> 4) & 1;
#pragma unroll
      for (int m = 0; m < 4; ++m) *(u32x2*)(a2b + (size_t)(ks * 8 + m) * 1024 + (half * 2 + (g >> 1)) * 256 + (g & 1) * 8) = (u32x2){hp[6][m][0], hp[6][m][1]};
    }
    ST_MARK(3); wait_vm<0>(); ST_FIN(4);
  } else {
    base.l0 = lds0 + 16384 + lb; base.l1 = base.l0 ^ 32;
    d.srd = make_srd(xn, S_ * D_ * 2); d.srdh = make_srd(w1, 1024u * 7168u);
    d.voff = lane * 16;
    const int pr = lane >> 1, r = pr ^ (((pr >> 3) & 1) << 2);
    d.voffh = r * 7168 + (lane & 1) * 16;
    d.so0 = (rb * 32 * 16 + 4 * wm) * 1024; d.step = 16384; d.so1 = hcol * 2; d.steph = 32 * 7168; d.so2 = 0;
    d.mask = 0xffffull << (16 * wm); d.ldsoff = lds0 + wm * 4096;
    if (wm < 2) dma16<0>(make_srd(wphi, H_ * 2048), lane * 16, hd * 2048 + wm * 1024, lds0 + G1_EXTRA + wm * 1024);
    g1_mainloop<1>(acc, base, d ST_PASS);
    ST_MARK(2);
    bar();
    bf16x8 wp[2], idm[2];
#pragma unroll
    for (int ft = 0; ft < 2; ++ft) {
      const unsigned a = lds0 + G1_EXTRA + (4 * g + q) * 64 + ft * 32 + p * 8;
      wp[ft] = cat(lds_tr(a), lds_tr(a + 1024));
      const unsigned one = (g == q) ? ((p & 1) ? 0x3f800000u : 0x00003f80u) : 0u;
      u32x4 iv = {0u, 0u, 0u, 0u};
      iv[ft * 2] = (p >> 1) == 0 ? one : 0u; iv[ft * 2 + 1] = (p >> 1) == 1 ? one : 0u;
      idm[ft] = __builtin_bit_cast(bf16x8, iv);
    }
    const f32x4 zero = {0.f, 0.f, 0.f, 0.f};
    const u32x4 onesu = {0x3f803f80u, 0x3f803f80u, 0x3f803f80u, 0x3f803f80u};
    const bf16x8 ones = __builtin_bit_cast(bf16x8, onesu);
    char* pqb = (char*)phiq + ((size_t)(hd * 128 + rb * 16 + wm * 4) * 2) * 1024 + lane * 16;
#pragma unroll
    for (int m = 0; m < 4; ++m) {
      const bf16x8 qf = pack8(acc[0][m], acc[1][m]);
      bf16x8 pos, neg;
      dual_softmax(MFMA(wp[0], qf, zero), MFMA(wp[1], qf, zero), pos, neg);
      *(bf16x8*)(pqb + (m * 2) * 1024) = pos; *(bf16x8*)(pqb + (m * 2 + 1) * 1024) = neg;
    }
    f32x4 kv[2][4], zz[4];
#pragma unroll
    for (int fq = 0; fq < 4; ++fq) { kv[0][fq] = zero; kv[1][fq] = zero; zz[fq] = zero; }
#pragma unroll
    for (int u = 0; u < 2; ++u) {
      f32x4 tr[2][4];
#pragma unroll
      for (int mm = 0; mm < 2; ++mm) {
        const int m = 2 * u + mm;
        const bf16x8 kf = pack8(acc[2][m], acc[3][m]);
        bf16x8 pos, neg;
        dual_softmax(MFMA(wp[0], kf, zero), MFMA(wp[1], kf, zero), pos, neg);
        tr[mm][0] = MFMA(pos, idm[0], zero); tr[mm][1] = MFMA(pos, idm[1], zero); tr[mm][2] = MFMA(neg, idm[0], zero); tr[mm][3] = MFMA(neg, idm[1], zero);
      }
      const bf16x8 v0 = pack8(acc[4][2 * u], acc[4][2 * u + 1]), v1 = pack8(acc[5][2 * u], acc[5][2 * u + 1]);
#pragma unroll
      for (int fq = 0; fq < 4; ++fq) {
        const bf16x8 ph = pack8(tr[0][fq], tr[1][fq]);
        kv[0][fq] = MFMA(v0, ph, kv[0][fq]); kv[1][fq] = MFMA(v1, ph, kv[1][fq]); zz[fq] = MFMA(ones, ph, zz[fq]);
      }
    }
    ST_MARK(3);
    LAS float* red = (LAS float*)(lds0 + wm * (KVP_STRIDE * 4));
    const int fl = lane & 15;
#pragma unroll
    for (int fq = 0; fq < 4; ++fq) {
      const int pn = fq >> 1, ft = fq & 1;
#pragma unroll
      for (int t = 0; t < 2; ++t)
#pragma unroll
        for (int r2 = 0; r2 < 4; ++r2) red[((pn * 2 + t) * 64 + (4 * g + r2) + 16 * (fl >> 2)) * 8 + ft * 4 + (fl & 3)] = kv[t][fq][r2];
      if (g == 0) red[2048 + (pn * 4 + (fl >> 2)) * 8 + ft * 4 + (fl & 3)] = zz[fq][0];
    }
    asm volatile("s_waitcnt lgkmcnt(0)" ::: "memory");
    bar();
    const int t2 = tid - 256;
    float* dst = kvp + (size_t)(hd * 8 + rb) * KVP_STRIDE;
#pragma unroll
    for (int i = 0; i < 3; ++i) {
      const int e = (t2 + 256 * i) * 4;
      if (e < KVP_STRIDE) {
        f32x4 s = *(const LAS f32x4*)(lds0 + e * 4);
#pragma unroll
        for (int w = 1; w < 4; ++w) s += *(const LAS f32x4*)(lds0 + w * (KVP_STRIDE * 4) + e * 4);
        *(f32x4*)(dst + e) = s;
      }
    }
    ST_MARK(4); wait_vm<0>(); ST_FIN(5);
  }
}

__global__ __launch_bounds__(256) void k_o(const u16* __restrict__ phiq, const float* __restrict__ kvp, u16* __restrict__ a2) {
  __shared__ __attribute__((aligned(16))) char sm[4096 + 128];
  const int tid = threadIdx.x, lane = tid & 63, wm = tid >> 6, g = lane >> 4;
  ST_INIT((char*)a2 - A2_OFF, 2);
  const int hd = blockIdx.x >> 3, rb = blockIdx.x & 7;
  const char* pqb = (const char*)phiq + ((size_t)(hd * 128 + rb * 16 + wm * 4) * 2) * 1024 + lane * 16;
  bf16x8 ph[4][2];
#pragma unroll
  for (int m = 0; m < 4; ++m) { ph[m][0] = *(const bf16x8*)(pqb + (m * 2) * 1024); ph[m][1] = *(const bf16x8*)(pqb + (m * 2 + 1) * 1024); }
  const float* src = kvp + (size_t)hd * 8 * KVP_STRIDE;
  f32x4 s0 = {0.f, 0.f, 0.f, 0.f}, s1 = s0;
#pragma unroll
  for (int b = 0; b < 8; ++b) { s0 += *(const f32x4*)(src + b * KVP_STRIDE + tid * 8); s1 += *(const f32x4*)(src + b * KVP_STRIDE + tid * 8 + 4); }
  *(bf16x8*)(sm + tid * 16) = pack8(s0, s1);
  if (tid < 8) {
    f32x4 z0 = {0.f, 0.f, 0.f, 0.f}, z1 = z0;
#pragma unroll
    for (int b = 0; b < 8; ++b) { z0 += *(const f32x4*)(src + b * KVP_STRIDE + 2048 + tid * 8); z1 += *(const f32x4*)(src + b * KVP_STRIDE + 2048 + tid * 8 + 4); }
    *(bf16x8*)(sm + 4096 + tid * 16) = pack8(z0, z1);
  }
  __syncthreads();
  bf16x8 kv[2][2], zf[2];
#pragma unroll
  for (int pn = 0; pn < 2; ++pn) { kv[pn][0] = *(const bf16x8*)(sm + (pn * 2) * 1024 + lane * 16); kv[pn][1] = *(const bf16x8*)(sm + (pn * 2 + 1) * 1024 + lane * 16); zf[pn] = *(const bf16x8*)(sm + 4096 + (pn * 4 + g) * 16); }
  const f32x4 zero = {0.f, 0.f, 0.f, 0.f};
  const int G = ((g & 1) << 1) | (g >> 1);
  const int mtg0 = rb * 16 + wm * 4;
  char* a2b = (char*)a2 + ((size_t)(mtg0 >> 3) * KS2 * 8 + (mtg0 & 7)) * 1024 + (lane & 15) * 16 + (size_t)(112 + hd) * 8 * 1024 + G * 256;
#pragma unroll
  for (int m = 0; m < 4; ++m) {
    f32x4 n0 = MFMA(kv[1][0], ph[m][1], MFMA(kv[0][0], ph[m][0], zero));
    f32x4 n1 = MFMA(kv[1][1], ph[m][1], MFMA(kv[0][1], ph[m][0], zero));
    f32x4 dn = MFMA(zf[1], ph[m][1], MFMA(zf[0], ph[m][0], zero));
    const float inv = 1.0f / (dn[0] + 1e-6f);
    *(u32x4*)(a2b + m * 1024) = frag_natural(cvtpk(n0[0] * inv, n0[1] * inv), cvtpk(n0[2] * inv, n0[3] * inv), cvtpk(n1[0] * inv, n1[1] * inv), cvtpk(n1[2] * inv, n1[3] * inv));
  }
  ST_MARK(1); wait_vm<0>(); ST_FIN(2);
}

constexpr int G2_SLOT = 24576, G2_NSLOT = 6, G2_LDS = G2_SLOT * G2_NSLOT;
struct G2Frag { bf16x8 a[4]; s16x4 b[2][2]; };
struct G2Dma { i32x4 srda, srdb; unsigned voffa, voffb, soa, sob, lda, ldb; };
template <int P> DI void g2_dma_piece(G2Dma& d, unsigned slot) {
  if constexpr (P == 0) dma16<0>(d.srda, d.voffa, d.soa, slot + d.lda);
  if constexpr (P == 1) { dma16<1024>(d.srda, d.voffa, d.soa, slot + d.lda); d.soa += 16384; }
  if constexpr (P == 2) { dma16w<0>(d.srdb, d.voffb, d.sob, slot + d.ldb); d.sob += 64 * 2048; }
}
template <int R> DI void g2_read(G2Frag& f, unsigned aa, unsigned l0, unsigned l1) {
  if constexpr (R < 2) f.b[0][R] = lds_tr(l0 + R * 256);
  else if constexpr (R < 6) f.a[R - 2] = lds_b128(aa + (R - 2) * 1024);
  else f.b[1][R - 6] = lds_tr(l1 + (R - 6) * 256);
}
template <bool DMA, bool READ, int I> DI void g2_slot(f32x4 (&acc)[2][4], const G2Frag& cur, G2Frag& nxt, unsigned aa, unsigned l0, unsigned l1, G2Dma& d, unsigned dslot) {
  constexpr int t = I >> 2, m = I & 3;
  acc[t][m] = MFMA(cat(cur.b[t][0], cur.b[t][1]), cur.a[m], acc[t][m]);
  if constexpr (READ) g2_read<I>(nxt, aa, l0, l1);
  if constexpr (DMA && I >= 5) g2_dma_piece<I - 5>(d, dslot);
  SB();
}
template <bool DMA, bool READ, int... I> DI void g2_body(f32x4 (&acc)[2][4], const G2Frag& cur, G2Frag& nxt, unsigned aa, unsigned l0, unsigned l1, G2Dma& d, unsigned dslot, std::integer_sequence<int, I...>) {
  (g2_slot<DMA, READ, I>(acc, cur, nxt, aa, l0, l1, d, dslot), ...);
}
DI unsigned ring2_next(unsigned s) { s += G2_SLOT; return s == G2_SLOT * G2_NSLOT ? 0u : s; }
template <bool DMA, bool READ, int VMSLOTS> DI void g2_step(f32x4 (&acc)[2][4], const G2Frag& cur, G2Frag& nxt, unsigned aa, unsigned l0, G2Dma& d, unsigned& rslot, unsigned& dslot) {
  if constexpr (READ) { wait_vm<VMSLOTS * 3>(); bar(); }
  const unsigned a = aa + rslot, b0 = l0 + rslot, b1 = (l0 ^ 32) + rslot;
  SB();
  g2_body<DMA, READ>(acc, cur, nxt, a, b0, b1, d, dslot, std::make_integer_sequence<int, 8>{});
  rslot = ring2_next(rslot); dslot = ring2_next(dslot);
}
__global__ __launch_bounds__(512, 2) void k_gemm2(const u16* __restrict__ a2, const u16* __restrict__ w2, const u16* __restrict__ wo, const u16* __restrict__ x,
                                                  const u16* __restrict__ b2, u16* __restrict__ out) {
  extern __shared__ __attribute__((aligned(16))) char smem[];
  const int tid = threadIdx.x, wid = __builtin_amdgcn_readfirstlane(tid >> 6), lane = tid & 63;
  ST_INIT((char*)a2 - A2_OFF, 3);
  const int kg = wid >> 2, wm = (wid >> 1) & 1, wn = wid & 1;
  const int xcd = blockIdx.x & 7, idx = blockIdx.x >> 3;
  const int rt = (xcd >> 1) * 4 + (idx & 3), ct = (xcd & 1) * 8 + (idx >> 2);
  const int g = lane >> 4, q = (lane & 15) >> 2, p = lane & 3;
  const unsigned lds0 = (unsigned)(size_t)(LAS char*)smem;
  const unsigned aa = lds0 + kg * 8192 + wm * 4096 + lane * 16;
  const unsigned l0 = lds0 + 16384 + kg * 4096 + wn * 2048 + (g >> 1) * 1024 + (g & 1) * 512 + q * 64 + (g & 1) * 32 + p * 8;
  G2Dma d;
  d.srda = make_srd(a2, (unsigned)A2_BYTES); d.srdb = make_srd(w2, F_ * 2048u);
  d.voffa = lane * 16; d.soa = (rt * KS2 * 8 + wid * 2) * 1024; d.lda = lds0 + wid * 2048;
  { const int r = lane >> 2, c = (lane & 3) ^ (((r >> 3) & 1) << 1); d.voffb = r * 2048 + c * 16; }
  const unsigned sob0 = ((wid >> 2) * 32 + (wid & 1) * 16) * 2048 + (ct * 64 + ((wid >> 1) & 1) * 32) * 2;
  d.sob = sob0; d.ldb = lds0 + 16384 + wid * 1024;
  f32x4 acc[2][4];
#pragma unroll
  for (int t = 0; t < 2; ++t)
#pragma unroll
    for (int m = 0; m < 4; ++m) acc[t][m] = (f32x4){0.f, 0.f, 0.f, 0.f};
#pragma unroll
  for (int s = 0; s < 5; ++s) { g2_dma_piece<0>(d, s * G2_SLOT); g2_dma_piece<1>(d, s * G2_SLOT); g2_dma_piece<2>(d, s * G2_SLOT); }
  G2Frag f0, f1;
  wait_vm<12>(); bar();
  ST_MARK(1);
  g2_read<0>(f0, aa, l0, l0 ^ 32); g2_read<1>(f0, aa, l0, l0 ^ 32); g2_read<2>(f0, aa, l0, l0 ^ 32); g2_read<3>(f0, aa, l0, l0 ^ 32);
  g2_read<4>(f0, aa, l0, l0 ^ 32); g2_read<5>(f0, aa, l0, l0 ^ 32); g2_read<6>(f0, aa, l0, l0 ^ 32); g2_read<7>(f0, aa, l0, l0 ^ 32);
  unsigned rslot = G2_SLOT, dslot = 5 * G2_SLOT;
  for (int s = 0; s < 66; s += 2) {
    g2_step<true, true, 3>(acc, f0, f1, aa, l0, d, rslot, dslot);
    if (s == 50) { d.srdb = make_srd(wo, 1024u * 2048u); d.sob = sob0; }
    g2_step<true, true, 3>(acc, f1, f0, aa, l0, d, rslot, dslot);
  }
  g2_step<true, true, 3>(acc, f0, f1, aa, l0, d, rslot, dslot);
  g2_step<false, true, 3>(acc, f1, f0, aa, l0, d, rslot, dslot);
  g2_step<false, true, 2>(acc, f0, f1, aa, l0, d, rslot, dslot);
  g2_step<false, true, 1>(acc, f1, f0, aa, l0, d, rslot, dslot);
  g2_step<false, true, 0>(acc, f0, f1, aa, l0, d, rslot, dslot);
  g2_step<false, false, 0>(acc, f1, f0, aa, l0, d, rslot, dslot);
  bar();
  ST_MARK(2);
  constexpr int PITCH = 68 * 4;
#pragma unroll
  for (int t = 0; t < 2; ++t)
#pragma unroll
    for (int m = 0; m < 4; ++m) *(LAS f32x4*)(lds0 + (kg * 128 + wm * 64 + m * 16 + (lane & 15)) * PITCH + (wn * 32 + t * 16 + g * 4) * 4) = acc[t][m];
  asm volatile("s_waitcnt lgkmcnt(0)" ::: "memory");
  bar();
#pragma unroll
  for (int i = 0; i < 2; ++i) {
    const int task = tid + 512 * i, row = task >> 3, c8 = (task & 7) * 8;
    const unsigned la = lds0 + row * PITCH + c8 * 4;
    f32x4 v0 = *(const LAS f32x4*)(la) + *(const LAS f32x4*)(la + 128 * PITCH);
    f32x4 v1 = *(const LAS f32x4*)(la + 16) + *(const LAS f32x4*)(la + 128 * PITCH + 16);
    const size_t go = (size_t)(rt * 128 + row) * D_ + ct * 64 + c8;
    const u32x4 xv = *(const u32x4*)(x + go), bv = *(const u32x4*)(b2 + ct * 64 + c8);
    u32x4 o;
    o[0] = cvtpk(v0[0] + lo_f(xv[0]) + lo_f(bv[0]), v0[1] + hi_f(xv[0]) + hi_f(bv[0]));
    o[1] = cvtpk(v0[2] + lo_f(xv[1]) + lo_f(bv[1]), v0[3] + hi_f(xv[1]) + hi_f(bv[1]));
    o[2] = cvtpk(v1[0] + lo_f(xv[2]) + lo_f(bv[2]), v1[1] + hi_f(xv[2]) + hi_f(bv[2]));
    o[3] = cvtpk(v1[2] + lo_f(xv[3]) + lo_f(bv[3]), v1[3] + hi_f(xv[3]) + hi_f(bv[3]));
    *(u32x4*)(out + go) = o;
  }
  ST_MARK(3); wait_vm<0>(); ST_FIN(4);
}

extern "C" void kernel_launch(void* const* d_in, const int* in_sizes, int n_in, void* d_out, int out_size, void* d_ws, size_t ws_size, hipStream_t stream) {
  const u16* x = (const u16*)d_in[0]; const u16* wqkv = (const u16*)d_in[1]; const u16* wphi = (const u16*)d_in[2]; const u16* wo = (const u16*)d_in[3];
  const u16* w1 = (const u16*)d_in[5]; const u16* b1 = (const u16*)d_in[6]; const u16* w2 = (const u16*)d_in[7]; const u16* b2 = (const u16*)d_in[8];
  char* ws = (char*)d_ws;
  u16* xn = (u16*)(ws + XN_OFF); u16* a2 = (u16*)(ws + A2_OFF); u16* phiq = (u16*)(ws + PHIQ_OFF); float* kvp = (float*)(ws + KVP_OFF);
  hipFuncSetAttribute((const void*)k_gemm1, hipFuncAttributeMaxDynamicSharedMemorySize, G1_LDS);
  hipFuncSetAttribute((const void*)k_gemm2, hipFuncAttributeMaxDynamicSharedMemorySize, G2_LDS);
  k_ln<<<256, 256, 0, stream>>>(x, xn, wqkv, w1, w2, wo);
  k_gemm1<<<256, 512, G1_LDS, stream>>>(xn, wqkv, wphi, w1, b1, a2, phiq, kvp);
  k_o<<<256, 256, 0, stream>>>(phiq, kvp, a2);
  k_gemm2<<<256, 512, G2_LDS, stream>>>(a2, w2, wo, x, b2, (u16*)d_out);
}
```

```cpp
#define WS_MB 100
#include <hip/hip_runtime.h>
#include <utility>

typedef unsigned short u16;
typedef __bf16 bf16x8 __attribute__((ext_vector_type(8)));
typedef __bf16 bf16x2 __attribute__((ext_vector_type(2)));
typedef short s16x4 __attribute__((ext_vector_type(4)));
typedef short s16x8 __attribute__((ext_vector_type(8)));
typedef float f32x2 __attribute__((ext_vector_type(2)));
typedef float f32x4 __attribute__((ext_vector_type(4)));
typedef unsigned u32x2 __attribute__((ext_vector_type(2)));
typedef unsigned u32x4 __attribute__((ext_vector_type(4)));
typedef int i32x4 __attribute__((ext_vector_type(4)));
#define LAS __attribute__((address_space(3)))
#define DI __device__ __forceinline__
#define SB() __builtin_amdgcn_sched_barrier(0)
#define MFMA(a, b, c) __builtin_amdgcn_mfma_f32_16x16x32_bf16((a), (b), (c), 0, 0, 0)

constexpr int S_ = 2048, D_ = 1024, H_ = 32, F_ = 3584;
constexpr int KS2 = 144;
constexpr size_t XN_OFF = 0;
constexpr size_t A2_OFF = 4u << 20;
constexpr size_t A2_BYTES = (size_t)S_ * 4608 * 2;
constexpr size_t PHIQ_OFF = 24u << 20;
constexpr size_t KVP_OFF = 32u << 20;
constexpr int KVP_STRIDE = 2112;

DI float bf2f(u16 b) { return __uint_as_float(((unsigned)b) << 16); }
DI unsigned cvtpk(float lo, float hi) { f32x2 v = {lo, hi}; bf16x2 b = __builtin_convertvector(v, bf16x2); return __builtin_bit_cast(unsigned, b); }
DI float rbf(float x) { return __uint_as_float(cvtpk(x, 0.f) << 16); }
DI float lo_f(unsigned p) { return __uint_as_float(p << 16); }
DI float hi_f(unsigned p) { return __uint_as_float(p & 0xffff0000u); }
DI bf16x8 pack8(const f32x4& a, const f32x4& b) { u32x4 r = {cvtpk(a[0], a[1]), cvtpk(a[2], a[3]), cvtpk(b[0], b[1]), cvtpk(b[2], b[3])}; return __builtin_bit_cast(bf16x8, r); }
DI i32x4 make_srd(const void* p, unsigned bytes) { unsigned long long a = (unsigned long long)p; i32x4 r; r[0] = (int)(unsigned)a; r[1] = (int)((unsigned)(a >> 32) & 0xffffu); r[2] = (int)bytes; r[3] = 0x00020000; return r; }

#define WNTS ""
template <int OFF> DI void dma16(const i32x4& srd, unsigned voff, unsigned soff, unsigned lds) {
  asm volatile("s_mov_b32 m0, %3\n\ts_nop 0\n\tbuffer_load_dwordx4 %0, %1, %2 offen offset:%4 lds" ::"v"(voff), "s"(srd), "s"(soff), "s"(lds), "i"(OFF) : "memory");
}
template <int OFF> DI void dma16w(const i32x4& srd, unsigned voff, unsigned soff, unsigned lds) {
  asm volatile("s_mov_b32 m0, %3\n\ts_nop 0\n\tbuffer_load_dwordx4 %0, %1, %2 offen offset:%4" WNTS " lds" ::"v"(voff), "s"(srd), "s"(soff), "s"(lds), "i"(OFF) : "memory");
}
DI void dma16_masked(const i32x4& srd, unsigned voff, unsigned soff, unsigned lds, unsigned long long mask) {
  asm volatile("s_mov_b32 m0, %3\n\ts_mov_b64 exec, %4\n\tbuffer_load_dwordx4 %0, %1, %2 offen" WNTS " lds\n\ts_mov_b64 exec, -1" ::"v"(voff), "s"(srd), "s"(soff), "s"(lds), "s"(mask) : "memory");
}
template <int N> DI void wait_vm() { asm volatile("s_waitcnt vmcnt(%0)" ::"i"(N) : "memory"); }
DI void bar() { asm volatile("" ::: "memory"); __builtin_amdgcn_s_barrier(); asm volatile("" ::: "memory"); }
DI bf16x8 lds_b128(unsigned addr) { return *(const LAS bf16x8*)(addr); }
DI s16x4 lds_tr(unsigned addr) { return __builtin_amdgcn_ds_read_tr16_b64_v4i16((LAS s16x4*)(addr)); }
DI bf16x8 cat(const s16x4& a, const s16x4& b) { return __builtin_bit_cast(bf16x8, __builtin_shufflevector(a, b, 0, 1, 2, 3, 4, 5, 6, 7)); }

#define ST_ARG
#define ST_PASS
#define ST_INIT(ws, k)
#define ST_MARK(i)
#define ST_FIN(i)
#define XLOAD(p) (*(p))
__global__ __launch_bounds__(256) void k_ln(const u16* __restrict__ x, u16* __restrict__ xn, const u16* wqkv, const u16* w1, const u16* w2, const u16* wo) {
  __shared__ float red[2][4][8];
  const int t = threadIdx.x, r = t & 7, c0 = t >> 3, w = t >> 6;
  ST_INIT(xn, 0);
  const int row = blockIdx.x * 8 + r;
  u32x4 v[4];
#pragma unroll
  for (int i = 0; i < 4; ++i) v[i] = XLOAD((const u32x4*)(x + (size_t)row * D_ + (c0 + 32 * i) * 8));
  float s = 0.f;
#pragma unroll
  for (int i = 0; i < 4; ++i)
#pragma unroll
    for (int j = 0; j < 4; ++j) s += lo_f(v[i][j]) + hi_f(v[i][j]);
  s += __shfl_xor(s, 8); s += __shfl_xor(s, 16); s += __shfl_xor(s, 32);
  if ((t & 63) < 8) red[0][w][r] = s;
  __syncthreads();
  const float mean = (red[0][0][r] + red[0][1][r] + red[0][2][r] + red[0][3][r]) * (1.f / D_);
  float q = 0.f;
#pragma unroll
  for (int i = 0; i < 4; ++i)
#pragma unroll
    for (int j = 0; j < 4; ++j) { float a = lo_f(v[i][j]) - mean, b = hi_f(v[i][j]) - mean; q += a * a + b * b; }
  q += __shfl_xor(q, 8); q += __shfl_xor(q, 16); q += __shfl_xor(q, 32);
  if ((t & 63) < 8) red[1][w][r] = q;
  __syncthreads();
  const float var = (red[1][0][r] + red[1][1][r] + red[1][2][r] + red[1][3][r]) * (1.f / D_);
  const float mb = rbf(mean);
  const float den = rbf(sqrtf(rbf(rbf(var) + 1e-5f)));
  const float inv = 1.0f / den;
  const int rb = row >> 8, mt = (row >> 4) & 15, rr = row & 15;
#pragma unroll
  for (int i = 0; i < 4; ++i) {
    u32x4 o;
#pragma unroll
    for (int j = 0; j < 4; ++j) {
      unsigned d = cvtpk(lo_f(v[i][j]) - mb, hi_f(v[i][j]) - mb);
      o[j] = cvtpk(lo_f(d) * inv, hi_f(d) * inv);
    }
    const int c = c0 + 32 * i, ks = c >> 2, g = c & 3;
    *(u32x4*)((char*)xn + ((size_t)((rb * 32 + ks) * 16 + mt) * 64 + g * 16 + rr) * 16) = o;
  }
  ST_MARK(1); wait_vm<0>(); ST_FIN(2);
}

#define G1_DMAPOS 0
constexpr int G1_SLOT = 29696, G1_NSLOT = 5, G1_EXTRA = G1_SLOT * G1_NSLOT, G1_LDS = G1_EXTRA + 4096;
struct G1Dma { i32x4 srd, srdh; unsigned voff, voffh, so0, so1, so2, step, steph; unsigned long long mask; unsigned ldsoff; };

template <int ROLE> DI void g1_dma(G1Dma& d, unsigned slot, int piece);
template <int ROLE, int P> DI void g1_dma_piece(G1Dma& d, unsigned slot) {
  if constexpr (ROLE == 0) {
    if constexpr (P == 0) { dma16w<0>(d.srd, d.voff, d.so0, slot + d.ldsoff); d.so0 += d.step; }
    if constexpr (P == 1) { dma16w<0>(d.srd, d.voff, d.so1, slot + d.ldsoff + 1024); d.so1 += d.step; }
    if constexpr (P == 2) { dma16w<0>(d.srd, d.voff, d.so2, slot + d.ldsoff + 2048); d.so2 += d.step; }
  } else {
    if constexpr (P < 4) { dma16<P * 1024>(d.srd, d.voff, d.so0, slot + d.ldsoff); if constexpr (P == 3) d.so0 += d.step; }
    if constexpr (P == 4) { dma16_masked(d.srdh, d.voffh, d.so1, slot + 28 * 1024, d.mask); d.so1 += d.steph; }
  }
}
template <int ROLE> DI void g1_dma_all(G1Dma& d, unsigned slot) {
  g1_dma_piece<ROLE, 0>(d, slot); g1_dma_piece<ROLE, 1>(d, slot); g1_dma_piece<ROLE, 2>(d, slot);
  if constexpr (ROLE == 1) { g1_dma_piece<ROLE, 3>(d, slot); g1_dma_piece<ROLE, 4>(d, slot); }
}
template <int ROLE> struct G1Frag { bf16x8 a[4]; s16x4 b[ROLE == 0 ? 7 : 6][2]; };
struct G1Addr { unsigned a, l0, l1, h0, h1; };
template <int ROLE, int R> DI void g1_read(G1Frag<ROLE>& f, const G1Addr& ad) {
  if constexpr (R < 2) { f.b[0][R] = lds_tr(ad.l0 + R * 256); }
  else if constexpr (R < 6) { f.a[R - 2] = lds_b128(ad.a + (R - 2) * 1024); }
  else {
    constexpr int t = (R - 4) >> 1, e = (R - 4) & 1;
    if constexpr (t < 6) f.b[t][e] = lds_tr(((t & 1) ? ad.l1 : ad.l0) + (t >> 1) * 2048 + e * 256);
    else f.b[t][e] = lds_tr(e ? ad.h1 : ad.h0);
  }
}
template <int ROLE, int... R> DI void g1_read_all(G1Frag<ROLE>& f, const G1Addr& ad, std::integer_sequence<int, R...>) { (g1_read<ROLE, R>(f, ad), ...); }

template <int ROLE, int I> DI void g1_mfma(f32x4 (&acc)[7][4], const G1Frag<ROLE>& f) {
  constexpr int t = I >> 2, m = I & 3;
  const bf16x8 b = cat(f.b[t][0], f.b[t][1]);
  if constexpr (ROLE == 1 && t >= 4) acc[t][m] = MFMA(f.a[m], b, acc[t][m]);
  else acc[t][m] = MFMA(b, f.a[m], acc[t][m]);
}
template <int ROLE, bool DMA, bool READ, int I> DI void g1_slot(f32x4 (&acc)[7][4], const G1Frag<ROLE>& cur, G1Frag<ROLE>& nxt, const G1Addr& ad, G1Dma& d, unsigned dslot) {
  constexpr int ND = ROLE == 0 ? 3 : 5, NR = ROLE == 0 ? 18 : 16;
  g1_mfma<ROLE, I>(acc, cur);
  if constexpr (READ && I < NR) g1_read<ROLE, I>(nxt, ad);
  if constexpr (DMA && I >= NR && I - NR < ND) g1_dma_piece<ROLE, I - NR>(d, dslot);
  SB();
}
template <int ROLE, bool DMA, bool READ, int... I> DI void g1_body(f32x4 (&acc)[7][4], const G1Frag<ROLE>& cur, G1Frag<ROLE>& nxt, const G1Addr& ad, G1Dma& d, unsigned dslot, std::integer_sequence<int, I...>) {
  (g1_slot<ROLE, DMA, READ, I>(acc, cur, nxt, ad, d, dslot), ...);
}
DI unsigned ring_next(unsigned s) { s += G1_SLOT; return s == G1_SLOT * G1_NSLOT ? 0u : s; }
template <int ROLE, bool DMA, bool READ, int VMSLOTS> DI void g1_step(f32x4 (&acc)[7][4], const G1Frag<ROLE>& cur, G1Frag<ROLE>& nxt, const G1Addr& base, G1Dma& d, unsigned& rslot, unsigned& dslot) {
  constexpr int ND = ROLE == 0 ? 3 : 5, NM = ROLE == 0 ? 28 : 24;
  if constexpr (READ) { wait_vm<VMSLOTS * ND>(); bar(); }
  G1Addr ad = {base.a + rslot, base.l0 + rslot, base.l1 + rslot, base.h0 + rslot, base.h1 + rslot};
  SB();
  g1_body<ROLE, DMA, READ>(acc, cur, nxt, ad, d, dslot, std::make_integer_sequence<int, NM>{});
  rslot = ring_next(rslot); dslot = ring_next(dslot);
}
template <int ROLE> DI void g1_mainloop(f32x4 (&acc)[7][4], const G1Addr& base, G1Dma& d ST_ARG) {
  constexpr int ND = ROLE == 0 ? 3 : 5, NR = ROLE == 0 ? 18 : 16;
  g1_dma_all<ROLE>(d, 0); g1_dma_all<ROLE>(d, G1_SLOT); g1_dma_all<ROLE>(d, 2 * G1_SLOT); g1_dma_all<ROLE>(d, 3 * G1_SLOT);
  G1Frag<ROLE> f0, f1;
  wait_vm<3 * ND>(); bar();
  ST_MARK(1);
  g1_read_all<ROLE>(f0, base, std::make_integer_sequence<int, NR>{});
  unsigned rslot = G1_SLOT, dslot = 4 * G1_SLOT;
  for (int s = 0; s < 28; s += 2) {
    g1_step<ROLE, true, true, 2>(acc, f0, f1, base, d, rslot, dslot);
    g1_step<ROLE, true, true, 2>(acc, f1, f0, base, d, rslot, dslot);
  }
  g1_step<ROLE, false, true, 2>(acc, f0, f1, base, d, rslot, dslot);
  g1_step<ROLE, false, true, 1>(acc, f1, f0, base, d, rslot, dslot);
  g1_step<ROLE, false, true, 0>(acc, f0, f1, base, d, rslot, dslot);
  g1_step<ROLE, false, false, 0>(acc, f1, f0, base, d, rslot, dslot);
}

DI float xmax(float v) { auto r = __builtin_amdgcn_permlane16_swap(__float_as_uint(v), __float_as_uint(v), false, false); v = fmaxf(__uint_as_float(r[0]), __uint_as_float(r[1]));
  auto q = __builtin_amdgcn_permlane32_swap(__float_as_uint(v), __float_as_uint(v), false, false); return fmaxf(__uint_as_float(q[0]), __uint_as_float(q[1])); }
DI float xsum(float v) { auto r = __builtin_amdgcn_permlane16_swap(__float_as_uint(v), __float_as_uint(v), false, false); v = __uint_as_float(r[0]) + __uint_as_float(r[1]);
  auto q = __builtin_amdgcn_permlane32_swap(__float_as_uint(v), __float_as_uint(v), false, false); return __uint_as_float(q[0]) + __uint_as_float(q[1]); }
DI void dual_softmax(const f32x4& p0, const f32x4& p1, bf16x8& pos, bf16x8& neg) {
  constexpr float L2E = 1.4426950408889634f;
  float p[8];
#pragma unroll
  for (int j = 0; j < 4; j += 2) { unsigned a = cvtpk(p0[j], p0[j + 1]), b = cvtpk(p1[j], p1[j + 1]); p[j] = lo_f(a); p[j + 1] = hi_f(a); p[4 + j] = lo_f(b); p[5 + j] = hi_f(b); }
  float mx = p[0], mn = p[0];
#pragma unroll
  for (int j = 1; j < 8; ++j) { mx = fmaxf(mx, p[j]); mn = fminf(mn, p[j]); }
  mx = xmax(mx); mn = -xmax(-mn);
  float ep[8], en[8], sp = 0.f, sn = 0.f;
#pragma unroll
  for (int j = 0; j < 8; ++j) { ep[j] = __builtin_amdgcn_exp2f((p[j] - mx) * L2E); en[j] = __builtin_amdgcn_exp2f((mn - p[j]) * L2E); sp += ep[j]; sn += en[j]; }
  sp = __builtin_amdgcn_rcpf(xsum(sp)); sn = __builtin_amdgcn_rcpf(xsum(sn));
  u32x4 a, b;
#pragma unroll
  for (int j = 0; j < 4; ++j) { a[j] = cvtpk(ep[2 * j] * sp, ep[2 * j + 1] * sp); b[j] = cvtpk(en[2 * j] * sn, en[2 * j + 1] * sn); }
  pos = __builtin_bit_cast(bf16x8, a); neg = __builtin_bit_cast(bf16x8, b);
}
DI u32x4 frag_natural(unsigned x0, unsigned x1, unsigned y0, unsigned y1) {
  auto r0 = __builtin_amdgcn_permlane16_swap(x0, y0, false, false);
  auto r1 = __builtin_amdgcn_permlane16_swap(x1, y1, false, false);
  u32x4 o = {r0[0], r1[0], r0[1], r1[1]};
  return o;
}

__global__ __launch_bounds__(512, 2) void k_gemm1(const u16* __restrict__ xn, const u16* __restrict__ wqkv, const u16* __restrict__ wphi, const u16* __restrict__ w1,
                                                  const u16* __restrict__ b1, u16* __restrict__ a2, u16* __restrict__ phiq, float* __restrict__ kvp) {
  extern __shared__ __attribute__((aligned(16))) char smem[];
  const int tid = threadIdx.x, wid = __builtin_amdgcn_readfirstlane(tid >> 6), lane = tid & 63, wm = wid & 3;
  ST_INIT(xn, 1);
  const int xcd = blockIdx.x & 7, idx = blockIdx.x >> 3;
  const int rb = (xcd >> 1) * 2 + (idx & 1), hd = (xcd & 1) * 16 + (idx >> 1);
  const int fc0 = 112 * hd, odd = hd & 1;
  const int gcol0 = fc0 + (odd ? 16 : 0), hcol = fc0 + (odd ? 0 : 96);
  const int g = lane >> 4, q = (lane & 15) >> 2, p = lane & 3;
  const unsigned lds0 = (unsigned)(size_t)(LAS char*)smem;
  f32x4 acc[7][4];
#pragma unroll
  for (int t = 0; t < 7; ++t)
#pragma unroll
    for (int m = 0; m < 4; ++m) acc[t][m] = (f32x4){0.f, 0.f, 0.f, 0.f};
  G1Addr base; G1Dma d;
  base.a = lds0 + wm * 4096 + lane * 16;
  const unsigned lb = (g >> 1) * 1024 + (g & 1) * 512 + q * 64 + (g & 1) * 32 + p * 8;
  base.h0 = lds0 + 28 * 1024 + g * 256 + (g & 1) * 128 + q * 32 + p * 8; base.h1 = base.h0 ^ 128;
  if (wid < 4) {
    base.l0 = lds0 + 16384 + 6144 + lb; base.l1 = base.l0 ^ 32;
    const bool isq = wid < 2;
    const unsigned pitch = isq ? 6144u : 7168u;
    d.srd = isq ? make_srd(wqkv, 1024u * 6144u) : make_srd(w1, 1024u * 7168u);
    d.srdh = d.srd; d.voffh = 0; d.steph = 0; d.mask = 0;
    const int r = lane >> 2, c = (lane & 3) ^ (((r >> 3) & 1) << 1);
    d.voff = r * pitch + c * 16;
    unsigned so[3];
#pragma unroll
    for (int i = 0; i < 3; ++i) { const int ub = 3 * wid + i, grp = ub >> 1, u = ub & 1; const int col = isq ? grp * 1024 + hd * 32 : gcol0 + (grp - 3) * 32; so[i] = u * 16 * pitch + col * 2; }
    d.so0 = so[0]; d.so1 = so[1]; d.so2 = so[2]; d.step = 32 * pitch; d.ldsoff = lds0 + 16384 + 3 * wid * 1024;
    if (wid == 0) dma16<0>(make_srd(b1, F_ * 2), lane * 16, fc0 * 2, lds0 + G1_EXTRA + 2048);
    g1_mainloop<0>(acc, base, d ST_PASS);
    ST_MARK(2);
    bar();
    const int G = ((g & 1) << 1) | (g >> 1);
    const int mtg0 = rb * 16 + wm * 4;
    char* a2b = (char*)a2 + ((size_t)(mtg0 >> 3) * KS2 * 8 + (mtg0 & 7)) * 1024 + (lane & 15) * 16;
    const unsigned bb = lds0 + G1_EXTRA + 2048 + g * 8;
    unsigned hp[7][4][2];
#pragma unroll
    for (int t = 0; t < 7; ++t) {
      const int cof = (t < 6) ? (gcol0 - fc0) + t * 16 : (hcol - fc0);
      const u32x2 bv = *(const LAS u32x2*)(bb + cof * 2);
      const float b0 = lo_f(bv[0]), b1v = hi_f(bv[0]), b2v = lo_f(bv[1]), b3 = hi_f(bv[1]);
#pragma unroll
      for (int m = 0; m < 4; ++m) {
        float v0 = fmaxf(acc[t][m][0] + b0, 0.f), v1 = fmaxf(acc[t][m][1] + b1v, 0.f), v2 = fmaxf(acc[t][m][2] + b2v, 0.f), v3 = fmaxf(acc[t][m][3] + b3, 0.f);
        hp[t][m][0] = cvtpk(v0 * v0, v1 * v1); hp[t][m][1] = cvtpk(v2 * v2, v3 * v3);
      }
    }
#pragma unroll
    for (int u = 0; u < 3; ++u) {
      const int ks = (gcol0 >> 5) + u;
#pragma unroll
      for (int m = 0; m < 4; ++m) *(u32x4*)(a2b + (size_t)(ks * 8 + m) * 1024 + G * 256) = frag_natural(hp[2 * u][m][0], hp[2 * u][m][1], hp[2 * u + 1][m][0], hp[2 * u + 1][m][1]);
    }
    {
      const int ks = hcol >> 5, half = (hcol >> 4) & 1;
#pragma unroll
      for (int m = 0; m < 4; ++m) *(u32x2*)(a2b + (size_t)(ks * 8 + m) * 1024 + (half * 2 + (g >> 1)) * 256 + (g & 1) * 8) = (u32x2){hp[6][m][0], hp[6][m][1]};
    }
    ST_MARK(3); wait_vm<0>(); ST_FIN(4);
  } else {
    base.l0 = lds0 + 16384 + lb; base.l1 = base.l0 ^ 32;
    d.srd = make_srd(xn, S_ * D_ * 2); d.srdh = make_srd(w1, 1024u * 7168u);
    d.voff = lane * 16;
    const int pr = lane >> 1, r = pr ^ (((pr >> 3) & 1) << 2);
    d.voffh = r * 7168 + (lane & 1) * 16;
    d.so0 = (rb * 32 * 16 + 4 * wm) * 1024; d.step = 16384; d.so1 = hcol * 2; d.steph = 32 * 7168; d.so2 = 0;
    d.mask = 0xffffull << (16 * wm); d.ldsoff = lds0 + wm * 4096;
    if (wm < 2) dma16<0>(make_srd(wphi, H_ * 2048), lane * 16, hd * 2048 + wm * 1024, lds0 + G1_EXTRA + wm * 1024);
    g1_mainloop<1>(acc, base, d ST_PASS);
    ST_MARK(2);
    bar();
    bf16x8 wp[2], idm[2];
#pragma unroll
    for (int ft = 0; ft < 2; ++ft) {
      const unsigned a = lds0 + G1_EXTRA + (4 * g + q) * 64 + ft * 32 + p * 8;
      wp[ft] = cat(lds_tr(a), lds_tr(a + 1024));
      const unsigned one = (g == q) ? ((p & 1) ? 0x3f800000u : 0x00003f80u) : 0u;
      u32x4 iv = {0u, 0u, 0u, 0u};
      iv[ft * 2] = (p >> 1) == 0 ? one : 0u; iv[ft * 2 + 1] = (p >> 1) == 1 ? one : 0u;
      idm[ft] = __builtin_bit_cast(bf16x8, iv);
    }
    const f32x4 zero = {0.f, 0.f, 0.f, 0.f};
    const u32x4 onesu = {0x3f803f80u, 0x3f803f80u, 0x3f803f80u, 0x3f803f80u};
    const bf16x8 ones = __builtin_bit_cast(bf16x8, onesu);
    char* pqb = (char*)phiq + ((size_t)(hd * 128 + rb * 16 + wm * 4) * 2) * 1024 + lane * 16;
#pragma unroll
    for (int m = 0; m < 4; ++m) {
      const bf16x8 qf = pack8(acc[0][m], acc[1][m]);
      bf16x8 pos, neg;
      dual_softmax(MFMA(wp[0], qf, zero), MFMA(wp[1], qf, zero), pos, neg);
      *(bf16x8*)(pqb + (m * 2) * 1024) = pos; *(bf16x8*)(pqb + (m * 2 + 1) * 1024) = neg;
    }
    f32x4 kv[2][4], zz[4];
#pragma unroll
    for (int fq = 0; fq < 4; ++fq) { kv[0][fq] = zero; kv[1][fq] = zero; zz[fq] = zero; }
#pragma unroll
    for (int u = 0; u < 2; ++u) {
      f32x4 tr[2][4];
#pragma unroll
      for (int mm = 0; mm < 2; ++mm) {
        const int m = 2 * u + mm;
        const bf16x8 kf = pack8(acc[2][m], acc[3][m]);
        bf16x8 pos, neg;
        dual_softmax(MFMA(wp[0], kf, zero), MFMA(wp[1], kf, zero), pos, neg);
        tr[mm][0] = MFMA(pos, idm[0], zero); tr[mm][1] = MFMA(pos, idm[1], zero); tr[mm][2] = MFMA(neg, idm[0], zero); tr[mm][3] = MFMA(neg, idm[1], zero);
      }
      const bf16x8 v0 = pack8(acc[4][2 * u], acc[4][2 * u + 1]), v1 = pack8(acc[5][2 * u], acc[5][2 * u + 1]);
#pragma unroll
      for (int fq = 0; fq < 4; ++fq) {
        const bf16x8 ph = pack8(tr[0][fq], tr[1][fq]);
        kv[0][fq] = MFMA(v0, ph, kv[0][fq]); kv[1][fq] = MFMA(v1, ph, kv[1][fq]); zz[fq] = MFMA(ones, ph, zz[fq]);
      }
    }
    ST_MARK(3);
    LAS float* red = (LAS float*)(lds0 + wm * (KVP_STRIDE * 4));
    const int fl = lane & 15;
#pragma unroll
    for (int fq = 0; fq < 4; ++fq) {
      const int pn = fq >> 1, ft = fq & 1;
#pragma unroll
      for (int t = 0; t < 2; ++t)
#pragma unroll
        for (int r2 = 0; r2 < 4; ++r2) red[((pn * 2 + t) * 64 + (4 * g + r2) + 16 * (fl >> 2)) * 8 + ft * 4 + (fl & 3)] = kv[t][fq][r2];
      if (g == 0) red[2048 + (pn * 4 + (fl >> 2)) * 8 + ft * 4 + (fl & 3)] = zz[fq][0];
    }
    asm volatile("s_waitcnt lgkmcnt(0)" ::: "memory");
    bar();
    const int t2 = tid - 256;
    float* dst = kvp + (size_t)(hd * 8 + rb) * KVP_STRIDE;
#pragma unroll
    for (int i = 0; i < 3; ++i) {
      const int e = (t2 + 256 * i) * 4;
      if (e < KVP_STRIDE) {
        f32x4 s = *(const LAS f32x4*)(lds0 + e * 4);
#pragma unroll
        for (int w = 1; w < 4; ++w) s += *(const LAS f32x4*)(lds0 + w * (KVP_STRIDE * 4) + e * 4);
        *(f32x4*)(dst + e) = s;
      }
    }
    ST_MARK(4); wait_vm<0>(); ST_FIN(5);
  }
}

__global__ __launch_bounds__(256) void k_o(const u16* __restrict__ phiq, const float* __restrict__ kvp, u16* __restrict__ a2) {
  __shared__ __attribute__((aligned(16))) char sm[4096 + 128];
  const int tid = threadIdx.x, lane = tid & 63, wm = tid >> 6, g = lane >> 4;
  ST_INIT((char*)a2 - A2_OFF, 2);
  const int hd = blockIdx.x >> 3, rb = blockIdx.x & 7;
  const char* pqb = (const char*)phiq + ((size_t)(hd * 128 + rb * 16 + wm * 4) * 2) * 1024 + lane * 16;
  bf16x8 ph[4][2];
#pragma unroll
  for (int m = 0; m < 4; ++m) { ph[m][0] = *(const bf16x8*)(pqb + (m * 2) * 1024); ph[m][1] = *(const bf16x8*)(pqb + (m * 2 + 1) * 1024); }
  const float* src = kvp + (size_t)hd * 8 * KVP_STRIDE;
  f32x4 s0 = {0.f, 0.f, 0.f, 0.f}, s1 = s0;
#pragma unroll
  for (int b = 0; b < 8; ++b) { s0 += *(const f32x4*)(src + b * KVP_STRIDE + tid * 8); s1 += *(const f32x4*)(src + b * KVP_STRIDE + tid * 8 + 4); }
  *(bf16x8*)(sm + tid * 16) = pack8(s0, s1);
  if (tid < 8) {
    f32x4 z0 = {0.f, 0.f, 0.f, 0.f}, z1 = z0;
#pragma unroll
    for (int b = 0; b < 8; ++b) { z0 += *(const f32x4*)(src + b * KVP_STRIDE + 2048 + tid * 8); z1 += *(const f32x4*)(src + b * KVP_STRIDE + 2048 + tid * 8 + 4); }
    *(bf16x8*)(sm + 4096 + tid * 16) = pack8(z0, z1);
  }
  __syncthreads();
  bf16x8 kv[2][2], zf[2];
#pragma unroll
  for (int pn = 0; pn < 2; ++pn) { kv[pn][0] = *(const bf16x8*)(sm + (pn * 2) * 1024 + lane * 16); kv[pn][1] = *(const bf16x8*)(sm + (pn * 2 + 1) * 1024 + lane * 16); zf[pn] = *(const bf16x8*)(sm + 4096 + (pn * 4 + g) * 16); }
  const f32x4 zero = {0.f, 0.f, 0.f, 0.f};
  const int G = ((g & 1) << 1) | (g >> 1);
  const int mtg0 = rb * 16 + wm * 4;
  char* a2b = (char*)a2 + ((size_t)(mtg0 >> 3) * KS2 * 8 + (mtg0 & 7)) * 1024 + (lane & 15) * 16 + (size_t)(112 + hd) * 8 * 1024 + G * 256;
#pragma unroll
  for (int m = 0; m < 4; ++m) {
    f32x4 n0 = MFMA(kv[1][0], ph[m][1], MFMA(kv[0][0], ph[m][0], zero));
    f32x4 n1 = MFMA(kv[1][1], ph[m][1], MFMA(kv[0][1], ph[m][0], zero));
    f32x4 dn = MFMA(zf[1], ph[m][1], MFMA(zf[0], ph[m][0], zero));
    const float inv = 1.0f / (dn[0] + 1e-6f);
    *(u32x4*)(a2b + m * 1024) = frag_natural(cvtpk(n0[0] * inv, n0[1] * inv), cvtpk(n0[2] * inv, n0[3] * inv), cvtpk(n1[0] * inv, n1[1] * inv), cvtpk(n1[2] * inv, n1[3] * inv));
  }
  ST_MARK(1); wait_vm<0>(); ST_FIN(2);
}

constexpr int G2_SLOT = 24576, G2_NSLOT = 6, G2_LDS = G2_SLOT * G2_NSLOT;
struct G2Frag { bf16x8 a[4]; s16x4 b[2][2]; };
struct G2Dma { i32x4 srda, srdb; unsigned voffa, voffb, soa, sob, lda, ldb; };
template <int P> DI void g2_dma_piece(G2Dma& d, unsigned slot) {
  if constexpr (P == 0) dma16<0>(d.srda, d.voffa, d.soa, slot + d.lda);
  if constexpr (P == 1) { dma16<1024>(d.srda, d.voffa, d.soa, slot + d.lda); d.soa += 16384; }
  if constexpr (P == 2) { dma16w<0>(d.srdb, d.voffb, d.sob, slot + d.ldb); d.sob += 64 * 2048; }
}
template <int R> DI void g2_read(G2Frag& f, unsigned aa, unsigned l0, unsigned l1) {
  if constexpr (R < 2) f.b[0][R] = lds_tr(l0 + R * 256);
  else if constexpr (R < 6) f.a[R - 2] = lds_b128(aa + (R - 2) * 1024);
  else f.b[1][R - 6] = lds_tr(l1 + (R - 6) * 256);
}
template <bool DMA, bool READ, int I> DI void g2_slot(f32x4 (&acc)[2][4], const G2Frag& cur, G2Frag& nxt, unsigned aa, unsigned l0, unsigned l1, G2Dma& d, unsigned dslot) {
  constexpr int t = I >> 2, m = I & 3;
  acc[t][m] = MFMA(cat(cur.b[t][0], cur.b[t][1]), cur.a[m], acc[t][m]);
  if constexpr (READ) g2_read<I>(nxt, aa, l0, l1);
  if constexpr (DMA && I >= 5) g2_dma_piece<I - 5>(d, dslot);
  SB();
}
template <bool DMA, bool READ, int... I> DI void g2_body(f32x4 (&acc)[2][4], const G2Frag& cur, G2Frag& nxt, unsigned aa, unsigned l0, unsigned l1, G2Dma& d, unsigned dslot, std::integer_sequence<int, I...>) {
  (g2_slot<DMA, READ, I>(acc, cur, nxt, aa, l0, l1, d, dslot), ...);
}
DI unsigned ring2_next(unsigned s) { s += G2_SLOT; return s == G2_SLOT * G2_NSLOT ? 0u : s; }
template <bool DMA, bool READ, int VMSLOTS> DI void g2_step(f32x4 (&acc)[2][4], const G2Frag& cur, G2Frag& nxt, unsigned aa, unsigned l0, G2Dma& d, unsigned& rslot, unsigned& dslot) {
  if constexpr (READ) { wait_vm<VMSLOTS * 3>(); bar(); }
  const unsigned a = aa + rslot, b0 = l0 + rslot, b1 = (l0 ^ 32) + rslot;
  SB();
  g2_body<DMA, READ>(acc, cur, nxt, a, b0, b1, d, dslot, std::make_integer_sequence<int, 8>{});
  rslot = ring2_next(rslot); dslot = ring2_next(dslot);
}
__global__ __launch_bounds__(512, 2) void k_gemm2(const u16* __restrict__ a2, const u16* __restrict__ w2, const u16* __restrict__ wo, const u16* __restrict__ x,
                                                  const u16* __restrict__ b2, u16* __restrict__ out) {
  extern __shared__ __attribute__((aligned(16))) char smem[];
  const int tid = threadIdx.x, wid = __builtin_amdgcn_readfirstlane(tid >> 6), lane = tid & 63;
  ST_INIT((char*)a2 - A2_OFF, 3);
  const int kg = wid >> 2, wm = (wid >> 1) & 1, wn = wid & 1;
  const int xcd = blockIdx.x & 7, idx = blockIdx.x >> 3;
  const int rt = (xcd >> 1) * 4 + (idx & 3), ct = (xcd & 1) * 8 + (idx >> 2);
  const int g = lane >> 4, q = (lane & 15) >> 2, p = lane & 3;
  const unsigned lds0 = (unsigned)(size_t)(LAS char*)smem;
  const unsigned aa = lds0 + kg * 8192 + wm * 4096 + lane * 16;
  const unsigned l0 = lds0 + 16384 + kg * 4096 + wn * 2048 + (g >> 1) * 1024 + (g & 1) * 512 + q * 64 + (g & 1) * 32 + p * 8;
  G2Dma d;
  d.srda = make_srd(a2, (unsigned)A2_BYTES); d.srdb = make_srd(w2, F_ * 2048u);
  d.voffa = lane * 16; d.soa = (rt * KS2 * 8 + wid * 2) * 1024; d.lda = lds0 + wid * 2048;
  { const int r = lane >> 2, c = (lane & 3) ^ (((r >> 3) & 1) << 1); d.voffb = r * 2048 + c * 16; }
  const unsigned sob0 = ((wid >> 2) * 32 + (wid & 1) * 16) * 2048 + (ct * 64 + ((wid >> 1) & 1) * 32) * 2;
  d.sob = sob0; d.ldb = lds0 + 16384 + wid * 1024;
  f32x4 acc[2][4];
#pragma unroll
  for (int t = 0; t < 2; ++t)
#pragma unroll
    for (int m = 0; m < 4; ++m) acc[t][m] = (f32x4){0.f, 0.f, 0.f, 0.f};
#pragma unroll
  for (int s = 0; s < 5; ++s) { g2_dma_piece<0>(d, s * G2_SLOT); g2_dma_piece<1>(d, s * G2_SLOT); g2_dma_piece<2>(d, s * G2_SLOT); }
  G2Frag f0, f1;
  wait_vm<12>(); bar();
  ST_MARK(1);
  g2_read<0>(f0, aa, l0, l0 ^ 32); g2_read<1>(f0, aa, l0, l0 ^ 32); g2_read<2>(f0, aa, l0, l0 ^ 32); g2_read<3>(f0, aa, l0, l0 ^ 32);
  g2_read<4>(f0, aa, l0, l0 ^ 32); g2_read<5>(f0, aa, l0, l0 ^ 32); g2_read<6>(f0, aa, l0, l0 ^ 32); g2_read<7>(f0, aa, l0, l0 ^ 32);
  unsigned rslot = G2_SLOT, dslot = 5 * G2_SLOT;
  for (int s = 0; s < 66; s += 2) {
    g2_step<true, true, 3>(acc, f0, f1, aa, l0, d, rslot, dslot);
    if (s == 50) { d.srdb = make_srd(wo, 1024u * 2048u); d.sob = sob0; }
    g2_step<true, true, 3>(acc, f1, f0, aa, l0, d, rslot, dslot);
  }
  g2_step<true, true, 3>(acc, f0, f1, aa, l0, d, rslot, dslot);
  g2_step<false, true, 3>(acc, f1, f0, aa, l0, d, rslot, dslot);
  g2_step<false, true, 2>(acc, f0, f1, aa, l0, d, rslot, dslot);
  g2_step<false, true, 1>(acc, f1, f0, aa, l0, d, rslot, dslot);
  g2_step<false, true, 0>(acc, f0, f1, aa, l0, d, rslot, dslot);
  g2_step<false, false, 0>(acc, f1, f0, aa, l0, d, rslot, dslot);
  bar();
  ST_MARK(2);
  constexpr int PITCH = 68 * 4;
#pragma unroll
  for (int t = 0; t < 2; ++t)
#pragma unroll
    for (int m = 0; m < 4; ++m) *(LAS f32x4*)(lds0 + (kg * 128 + wm * 64 + m * 16 + (lane & 15)) * PITCH + (wn * 32 + t * 16 + g * 4) * 4) = acc[t][m];
  asm volatile("s_waitcnt lgkmcnt(0)" ::: "memory");
  bar();
#pragma unroll
  for (int i = 0; i < 2; ++i) {
    const int task = tid + 512 * i, row = task >> 3, c8 = (task & 7) * 8;
    const unsigned la = lds0 + row * PITCH + c8 * 4;
    f32x4 v0 = *(const LAS f32x4*)(la) + *(const LAS f32x4*)(la + 128 * PITCH);
    f32x4 v1 = *(const LAS f32x4*)(la + 16) + *(const LAS f32x4*)(la + 128 * PITCH + 16);
    const size_t go = (size_t)(rt * 128 + row) * D_ + ct * 64 + c8;
    const u32x4 xv = *(const u32x4*)(x + go), bv = *(const u32x4*)(b2 + ct * 64 + c8);
    u32x4 o;
    o[0] = cvtpk(v0[0] + lo_f(xv[0]) + lo_f(bv[0]), v0[1] + hi_f(xv[0]) + hi_f(bv[0]));
    o[1] = cvtpk(v0[2] + lo_f(xv[1]) + lo_f(bv[1]), v0[3] + hi_f(xv[1]) + hi_f(bv[1]));
    o[2] = cvtpk(v1[0] + lo_f(xv[2]) + lo_f(bv[2]), v1[1] + hi_f(xv[2]) + hi_f(bv[2]));
    o[3] = cvtpk(v1[2] + lo_f(xv[3]) + lo_f(bv[3]), v1[3] + hi_f(xv[3]) + hi_f(bv[3]));
    *(u32x4*)(out + go) = o;
  }
  ST_MARK(3); wait_vm<0>(); ST_FIN(4);
}

__global__ __launch_bounds__(512) void k_dummy(char* g) {
  u32x4 v = {1u, 2u, 3u, threadIdx.x};
#pragma unroll
  for (int j = 0; j < 16; ++j) *(u32x4*)(g + (size_t)blockIdx.x * 131072 + j * 8192 + threadIdx.x * 16) = v;
}
extern "C" void kernel_launch(void* const* d_in, const int* in_sizes, int n_in, void* d_out, int out_size, void* d_ws, size_t ws_size, hipStream_t stream) {
  const u16* x = (const u16*)d_in[0]; const u16* wqkv = (const u16*)d_in[1]; const u16* wphi = (const u16*)d_in[2]; const u16* wo = (const u16*)d_in[3];
  const u16* w1 = (const u16*)d_in[5]; const u16* b1 = (const u16*)d_in[6]; const u16* w2 = (const u16*)d_in[7]; const u16* b2 = (const u16*)d_in[8];
  char* ws = (char*)d_ws + ((size_t)WS_MB << 20);
  u16* xn = (u16*)(ws + XN_OFF); u16* a2 = (u16*)(ws + A2_OFF); u16* phiq = (u16*)(ws + PHIQ_OFF); float* kvp = (float*)(ws + KVP_OFF);
  hipFuncSetAttribute((const void*)k_gemm1, hipFuncAttributeMaxDynamicSharedMemorySize, G1_LDS);
  hipFuncSetAttribute((const void*)k_gemm2, hipFuncAttributeMaxDynamicSharedMemorySize, G2_LDS);
  k_ln<<<256, 256, 0, stream>>>(x, xn, wqkv, w1, w2, wo);
  k_gemm1<<<256, 512, G1_LDS, stream>>>(xn, wqkv, wphi, w1, b1, a2, phiq, kvp);
  k_o<<<256, 256, 0, stream>>>(phiq, kvp, a2);
  k_gemm2<<<256, 512, G2_LDS, stream>>>(a2, w2, wo, x, b2, (u16*)d_out);
}
```

```cpp
#include <hip/hip_runtime.h>
#include <utility>
#include <cstdio>

typedef unsigned short u16;
typedef __bf16 bf16x8 __attribute__((ext_vector_type(8)));
typedef __bf16 bf16x2 __attribute__((ext_vector_type(2)));
typedef short s16x4 __attribute__((ext_vector_type(4)));
typedef float f32x2 __attribute__((ext_vector_type(2)));
typedef float f32x4 __attribute__((ext_vector_type(4)));
typedef unsigned u32x2 __attribute__((ext_vector_type(2)));
typedef unsigned u32x4 __attribute__((ext_vector_type(4)));
typedef int i32x4 __attribute__((ext_vector_type(4)));
#define LAS __attribute__((address_space(3)))
#define GAS __attribute__((address_space(1)))
#define DI __device__ __forceinline__
#define SB() __builtin_amdgcn_sched_barrier(0)
#define MFMA(a, b, c) __builtin_amdgcn_mfma_f32_16x16x32_bf16((a), (b), (c), 0, 0, 0)

constexpr int S_ = 2048, D_ = 1024, H_ = 32, F_ = 3584;
constexpr int KS2 = 144;
constexpr size_t XN_OFF = 0;
constexpr size_t A2_OFF = 4u << 20;
constexpr size_t A2_BYTES = (size_t)S_ * 4608 * 2;
constexpr size_t KVP_OFF = 32u << 20;
constexpr int KVP_STRIDE = 2112;
constexpr size_t XCH_OFF = 36u << 20;
constexpr size_t FLG_OFF = 52u << 20;

DI unsigned cvtpk(float lo, float hi) { f32x2 v = {lo, hi}; bf16x2 b = __builtin_convertvector(v, bf16x2); return __builtin_bit_cast(unsigned, b); }
DI float rbf(float x) { return __uint_as_float(cvtpk(x, 0.f) << 16); }
DI float lo_f(unsigned p) { return __uint_as_float(p << 16); }
DI float hi_f(unsigned p) { return __uint_as_float(p & 0xffff0000u); }
DI bf16x8 pack8(const f32x4& a, const f32x4& b) { u32x4 r = {cvtpk(a[0], a[1]), cvtpk(a[2], a[3]), cvtpk(b[0], b[1]), cvtpk(b[2], b[3])}; return __builtin_bit_cast(bf16x8, r); }
DI i32x4 make_srd(const void* p, unsigned bytes) { unsigned long long a = (unsigned long long)p; i32x4 r; r[0] = (int)(unsigned)a; r[1] = (int)((unsigned)(a >> 32) & 0xffffu); r[2] = (int)bytes; r[3] = 0x00020000; return r; }

template <int OFF> DI void dma16(const i32x4& srd, unsigned voff, unsigned soff, unsigned lds) {
  asm volatile("s_mov_b32 m0, %3\n\ts_nop 0\n\tbuffer_load_dwordx4 %0, %1, %2 offen offset:%4 lds" ::"v"(voff), "s"(srd), "s"(soff), "s"(lds), "i"(OFF) : "memory");
}
template <int IMM> DI void set_m0(unsigned base) { asm volatile("s_add_u32 m0, %0, %1" ::"s"(base), "i"(IMM) : "scc"); }
template <int OFF> DI void dma_ld(const i32x4& srd, unsigned voff, unsigned soff) {
  asm volatile("buffer_load_dwordx4 %0, %1, %2 offen offset:%3 lds" ::"v"(voff), "s"(srd), "s"(soff), "i"(OFF) : "memory");
}
template <int OFF> DI void dma_ld_nt(const i32x4& srd, unsigned voff, unsigned soff) {
  asm volatile("buffer_load_dwordx4 %0, %1, %2 offen offset:%3 nt lds" ::"v"(voff), "s"(srd), "s"(soff), "i"(OFF) : "memory");
}
DI void nop1() { asm volatile("s_nop 0"); }
DI void touch(const i32x4& srd, unsigned voff, unsigned soff, unsigned lds) {
  asm volatile("s_mov_b32 m0, %3\n\ts_nop 0\n\tbuffer_load_dword %0, %1, %2 offen lds" ::"v"(voff), "s"(srd), "s"(soff), "s"(lds) : "memory");
}
constexpr unsigned OOB = 0x7fffffffu;
template <int N> DI void wait_vm() { asm volatile("s_waitcnt vmcnt(%0)" ::"i"(N) : "memory"); }
DI void wait_lgkm0() { __builtin_amdgcn_s_waitcnt(0xC07F); }
DI void bar() { asm volatile("" ::: "memory"); __builtin_amdgcn_s_barrier(); asm volatile("" ::: "memory"); }
DI bf16x8 lds_b128(unsigned addr) { return *(const LAS bf16x8*)(addr); }
DI s16x4 lds_tr(unsigned addr) { return __builtin_amdgcn_ds_read_tr16_b64_v4i16((LAS s16x4*)(addr)); }
DI bf16x8 cat(const s16x4& a, const s16x4& b) { return __builtin_bit_cast(bf16x8, __builtin_shufflevector(a, b, 0, 1, 2, 3, 4, 5, 6, 7)); }

#define ST_ARG
#define ST_PASS
#define ST_INIT(ws, k)
#define ST_MARK(i)
#define ST_FIN(i)

DI int head_pair(int xc, int tl) { return ((7 * xc + (tl < 2 ? 7 : 3)) & 7) + 8 * (tl & 1); }
DI unsigned qkv_col(int xc, int c) { return (c >> 1) * 2048 + head_pair(xc, (c < 2 ? 2 : 0) + (c & 1)) * 128; }
DI unsigned qkv_lines(int xc, int i, int lane) { const int r = lane / 6, c = lane % 6; return lane < 48 ? (unsigned)((r * 32 + i) * 6144) + qkv_col(xc, c) : OOB; }
DI unsigned w1_lines(int xc, int i, int lane) { const int r = lane / 7, c = lane % 7; return lane < 56 ? (unsigned)((r * 32 + i) * 7168 + xc * 896 + c * 128) : OOB; }
#define G1_PF0 1
__global__ __launch_bounds__(256) void k_ln(const u16* __restrict__ x, char* __restrict__ ws, const u16* __restrict__ wqkv, const u16* __restrict__ w1,
                                            const u16* __restrict__ wphi, const u16* __restrict__ b1) {
  __shared__ float red[2][4][8];
  __shared__ unsigned scratch[64];
  const int t = threadIdx.x, r = t & 7, c0 = t >> 3, w = t >> 6;
  ST_INIT(ws, 0);
  const int row = blockIdx.x * 8 + r;
  u32x4 v[4];
#pragma unroll
  for (int i = 0; i < 4; ++i) v[i] = *(const u32x4*)(x + (size_t)row * D_ + (c0 + 32 * i) * 8);
  {
    const int xc = blockIdx.x & 7, idx = blockIdx.x >> 3, lane = t & 63, wv = __builtin_amdgcn_readfirstlane(w);
    const unsigned sc = (unsigned)(size_t)(LAS unsigned*)scratch;
    if (wv == 0) { for (int j = 0; j < G1_PF0; ++j) touch(make_srd(wqkv, 1024u * 6144u), qkv_lines(xc, idx, lane), j * 256 * 6144, sc); }
    else if (wv == 1) { for (int j = 0; j < G1_PF0; ++j) touch(make_srd(w1, 1024u * 7168u), w1_lines(xc, idx, lane), j * 256 * 7168, sc); }
    else if (wv == 2 && idx < 4) touch(make_srd(wphi, H_ * 2048), lane < 32 ? lane * 128u : OOB, head_pair(xc, idx) * 4096, sc);
    else if (wv == 3 && idx == 0) touch(make_srd(b1, F_ * 2), lane < 7 ? lane * 128u : OOB, xc * 896, sc);
  }
  if (t == 0) ((unsigned*)(ws + FLG_OFF))[(16 + blockIdx.x) * 64] = 0u;
  if (t == 1 && blockIdx.x < 16) ((unsigned*)(ws + FLG_OFF))[blockIdx.x * 64] = 0u;
  float s = 0.f;
#pragma unroll
  for (int i = 0; i < 4; ++i)
#pragma unroll
    for (int j = 0; j < 4; ++j) s += lo_f(v[i][j]) + hi_f(v[i][j]);
  s += __shfl_xor(s, 8); s += __shfl_xor(s, 16); s += __shfl_xor(s, 32);
  if ((t & 63) < 8) red[0][w][r] = s;
  __syncthreads();
  const float mean = (red[0][0][r] + red[0][1][r] + red[0][2][r] + red[0][3][r]) * (1.f / D_);
  float q = 0.f;
#pragma unroll
  for (int i = 0; i < 4; ++i)
#pragma unroll
    for (int j = 0; j < 4; ++j) { float a = lo_f(v[i][j]) - mean, b = hi_f(v[i][j]) - mean; q += a * a + b * b; }
  q += __shfl_xor(q, 8); q += __shfl_xor(q, 16); q += __shfl_xor(q, 32);
  if ((t & 63) < 8) red[1][w][r] = q;
  __syncthreads();
  const float var = (red[1][0][r] + red[1][1][r] + red[1][2][r] + red[1][3][r]) * (1.f / D_);
  const float mb = rbf(mean);
  const float den = rbf(sqrtf(rbf(rbf(var) + 1e-5f)));
  const float inv = 1.0f / den;
  const int rb = row >> 8, mt = (row >> 4) & 15, rr = row & 15;
#pragma unroll
  for (int i = 0; i < 4; ++i) {
    u32x4 o;
#pragma unroll
    for (int j = 0; j < 4; ++j) {
      unsigned d = cvtpk(lo_f(v[i][j]) - mb, hi_f(v[i][j]) - mb);
      o[j] = cvtpk(lo_f(d) * inv, hi_f(d) * inv);
    }
    const int c = c0 + 32 * i, ks = c >> 2, g = c & 3;
    *(u32x4*)(ws + XN_OFF + ((size_t)((ks * 8 + rb) * 16 + mt) * 64 + g * 16 + rr) * 16) = o;
  }
  ST_MARK(1);
  ST_FIN(2);
}

DI void mfma_acc(f32x4& acc, const bf16x8& a, const bf16x8& b) { asm volatile("v_mfma_f32_16x16x32_bf16 %0, %1, %2, %0" : "+v"(acc) : "v"(a), "v"(b)); }
DI void acc_fence() { SB(); asm volatile("s_nop 7\n\ts_nop 7"); SB(); }
template <int NT> struct Frag { bf16x8 a[4]; s16x4 b[NT][2]; };
struct Adr { unsigned a, l0, l1, lx; };
struct Dma { i32x4 sa, sb; unsigned va0, va1, vb0, vb1, soa, sob, stepa, stepb, lda, ldb; };
#define NT1 0
#define NT2 0
#define PF2LATE 0
#define KTAIL 3
template <int NT, int LSTR, int RO, int R> DI void k_read(Frag<NT>& f, const Adr& ad) {
  if constexpr (R < 2) f.b[0][R] = lds_tr(ad.l0 + RO + R * 256);
  else if constexpr (R < 6) f.a[R - 2] = lds_b128(ad.a + RO + (R - 2) * 1024);
  else {
    constexpr int t = (R - 4) >> 1, e = (R - 4) & 1;
    if constexpr (t < 6) f.b[t][e] = lds_tr(((t & 1) ? ad.l1 : ad.l0) + RO + (t >> 1) * LSTR + e * 256);
    else f.b[t][e] = lds_tr(ad.lx + RO + e * 256);
  }
}
template <int NT, int LSTR, int RO, int... R> DI void k_read_all(Frag<NT>& f, const Adr& ad, std::integer_sequence<int, R...>) { (k_read<NT, LSTR, RO, R>(f, ad), ...); }
template <int NB, int WO, int P> DI void k_dma(Dma& d) {
  if constexpr (P == 0) dma_ld<0>(d.sa, d.va0, d.soa);
  if constexpr (P == 1) { dma_ld<1024>(d.sa, d.va1, d.soa); d.soa += d.stepa; if constexpr (NB > 0) set_m0<WO>(d.ldb); }
  if constexpr (P == 2) { if constexpr ((NB == 1 && NT2) || (NB == 2 && NT1)) dma_ld_nt<0>(d.sb, d.vb0, d.sob); else dma_ld<0>(d.sb, d.vb0, d.sob); if constexpr (NB == 1) d.sob += d.stepb; }
  if constexpr (P == 3) { if constexpr (NT1) dma_ld_nt<2048>(d.sb, d.vb1, d.sob); else dma_ld<2048>(d.sb, d.vb1, d.sob); d.sob += d.stepb; }
}
template <int NB, int WO> DI void k_dma_all(Dma& d) {
  set_m0<WO>(d.lda); nop1(); k_dma<NB, WO, 0>(d); k_dma<NB, WO, 1>(d);
  if constexpr (NB > 0) { nop1(); k_dma<NB, WO, 2>(d); }
  if constexpr (NB > 1) k_dma<NB, WO, 3>(d);
}
template <int NT, int NB, int LSTR, int RO, int WO, int J> DI void k_memop(Frag<NT>& n, const Adr& ad, Dma& d) {
  constexpr int ND = 2 + NB, TOT = 4 + 2 * NT + ND, before = (J * ND) / TOT, after = ((J + 1) * ND) / TOT;
  if constexpr (after > before) k_dma<NB, WO, before>(d); else k_read<NT, LSTR, RO, J - before>(n, ad);
  if constexpr (J == 0) set_m0<WO>(d.lda);
}
template <int NT, int NB, bool VT, int LSTR, int RO, int WO, int VM, int I, int... J>
DI void k_gap(f32x4 (&acc)[NT][4], const Frag<NT>& c, Frag<NT>& n, const Adr& ad, Dma& d, std::integer_sequence<int, J...>) {
  constexpr int NM = 4 * NT, ND = 2 + NB, TOT = 4 + 2 * NT + ND, SPAN = NM - KTAIL;
  constexpr int t = I >> 2, m = (t & 1) ? 3 - (I & 3) : (I & 3);
  if constexpr (VT && (t == 2 || t == 3)) mfma_acc(acc[t][m], c.a[m], cat(c.b[t][0], c.b[t][1]));
  else mfma_acc(acc[t][m], cat(c.b[t][0], c.b[t][1]), c.a[m]);
  SB();
  if constexpr (I == NM - 1) __builtin_amdgcn_s_setprio(3);
  if constexpr (I == SPAN / 3) __builtin_amdgcn_s_setprio(2);
  if constexpr (I == 2 * SPAN / 3) __builtin_amdgcn_s_setprio(1);
  if constexpr (I == SPAN) __builtin_amdgcn_s_setprio(0);
  (((J * SPAN / TOT == I) ? k_memop<NT, NB, LSTR, RO, WO, J>(n, ad, d) : (void)0), ...);
  if constexpr (I == NM - 2) { wait_lgkm0(); wait_vm<VM * ND>(); }
  SB();
}
template <int NT, int NB, bool VT, int LSTR, int RO, int WO, int VM, int... I>
DI void k_gaps(f32x4 (&acc)[NT][4], const Frag<NT>& c, Frag<NT>& n, const Adr& ad, Dma& d, std::integer_sequence<int, I...>) {
  (k_gap<NT, NB, VT, LSTR, RO, WO, VM, I>(acc, c, n, ad, d, std::make_integer_sequence<int, 4 + 2 * NT + 2 + NB>{}), ...);
}
template <int NT, int NB, bool VT, int LSTR, int RO, int WO, int VM> DI void k_step(f32x4 (&acc)[NT][4], const Frag<NT>& c, Frag<NT>& n, const Adr& ad, Dma& d) {
  bar(); SB();
  k_gaps<NT, NB, VT, LSTR, RO, WO, VM>(acc, c, n, ad, d, std::make_integer_sequence<int, 4 * NT>{});
}
DI Adr adr_at(const Adr& b, unsigned o) { Adr r = {b.a + o, b.l0 + o, b.l1 + o, b.lx + o}; asm volatile("" : "+v"(r.a), "+v"(r.l0), "+v"(r.l1), "+v"(r.lx)); return r; }
constexpr unsigned BIAS = 4096;
DI i32x4 make_srd_b(const void* p, unsigned bytes) { return make_srd((const char*)p - BIAS, bytes + BIAS); }

constexpr int G1_SLOT = 31744, G1_EXTRA = G1_SLOT * 4, G1_SCR = G1_EXTRA + 5120, G1_LDS = G1_SCR + 256;
struct Pf { i32x4 sq, s1, s2, so; unsigned vq, v1; int o2, i; };
template <int NT, int NB, bool KV> DI void g1_mainloop(f32x4 (&acc)[NT][4], const Adr& ad0, Dma& d, const Pf& pf, int lane ST_ARG) {
  constexpr int ND = 2 + NB;
  k_dma_all<NB, 0>(d); k_dma_all<NB, G1_SLOT>(d); k_dma_all<NB, 2 * G1_SLOT>(d); k_dma_all<NB, 3 * G1_SLOT>(d);
  const Adr ad1 = adr_at(ad0, 2 * G1_SLOT);
  Frag<NT> f0, f1;
  wait_vm<3 * ND>(); bar();
  k_read_all<NT, 4096, 0>(f0, ad0, std::make_integer_sequence<int, 4 + 2 * NT>{});
  wait_lgkm0(); wait_vm<2 * ND>();
  ST_MARK(1);
  for (int it = 0; it < 8; ++it) {
    k_step<NT, NB, KV, 4096, G1_SLOT, 0, 2>(acc, f0, f1, ad0, d);
    k_step<NT, NB, KV, 4096, 0, G1_SLOT, 2>(acc, f1, f0, ad1, d);
    k_step<NT, NB, KV, 4096, G1_SLOT, 2 * G1_SLOT, 2>(acc, f0, f1, ad1, d);
    k_step<NT, NB, KV, 4096, 0, 3 * G1_SLOT, 2>(acc, f1, f0, ad0, d);
    if constexpr (NB == 0) {
      if (pf.i >= 0 && (pf.i >> 2) == it && pf.i + 8 * G1_PF0 < 32) {
#pragma unroll
        for (int j = 0; j < 4; ++j) { touch(pf.sq, pf.vq, ((pf.i + 8 * G1_PF0) * 32 + j * 8) * 6144, G1_SCR); touch(pf.s1, pf.v1, ((pf.i + 8 * G1_PF0) * 32 + j * 8) * 7168, G1_SCR); }
      }
      if (pf.i >= 0 && (pf.i < 24 ? pf.i >> 2 : ((pf.i - 24) * 3) >> 2) == it) {
        const int n = pf.i < 24 ? 3 : 9, p0 = pf.o2 + (pf.i < 24 ? pf.i * 3 : 72 + (pf.i - 24) * 9);
        for (int j = 0; j < n; ++j) { const int pc = p0 + j; if (pc < 896) touch(pf.s2, lane * 128, pc * 8192, G1_SCR); else touch(pf.so, lane * 128, (pc - 896) * 8192, G1_SCR); }
      }
    }
  }
  __builtin_amdgcn_s_setprio(0);
  acc_fence();
  wait_vm<0>();
}

DI float max3f(float a, float b, float c) { return __builtin_fmaxf(__builtin_fmaxf(a, b), c); }
DI float max2f(float a, float b) { return __builtin_fmaxf(a, b); }
DI void swap16(float& a, float& b) { auto r = __builtin_amdgcn_permlane16_swap(__float_as_uint(a), __float_as_uint(b), false, false); a = __uint_as_float(r[0]); b = __uint_as_float(r[1]); }
DI void swap32(float& a, float& b) { auto r = __builtin_amdgcn_permlane32_swap(__float_as_uint(a), __float_as_uint(b), false, false); a = __uint_as_float(r[0]); b = __uint_as_float(r[1]); }
DI void dual_exp(const f32x4 (&p)[4][2], bf16x8 (&pos)[4], bf16x8 (&neg)[4]) {
  constexpr float L2E = 1.4426950408889634f;
  float mx[4], nm[4];
#pragma unroll
  for (int m = 0; m < 4; ++m) {
    mx[m] = max2f(max3f(max3f(p[m][0][0], p[m][0][1], p[m][0][2]), p[m][0][3], p[m][1][0]), max3f(p[m][1][1], p[m][1][2], p[m][1][3]));
    nm[m] = max2f(max3f(max3f(-p[m][0][0], -p[m][0][1], -p[m][0][2]), -p[m][0][3], -p[m][1][0]), max3f(-p[m][1][1], -p[m][1][2], -p[m][1][3]));
  }
  float a[4];
#pragma unroll
  for (int m = 0; m < 4; ++m) { swap16(mx[m], nm[m]); a[m] = max2f(mx[m], nm[m]); }
  swap32(a[0], a[1]); swap32(a[2], a[3]);
  float b0 = max2f(a[0], a[1]) * -L2E, b1 = max2f(a[2], a[3]) * -L2E;
  a[0] = b0; a[1] = b0; a[2] = b1; a[3] = b1;
  swap32(a[0], a[1]); swap32(a[2], a[3]);
#pragma unroll
  for (int m = 0; m < 4; ++m) { mx[m] = a[m]; nm[m] = a[m]; swap16(mx[m], nm[m]); }
#pragma unroll
  for (int m = 0; m < 4; ++m) {
    f32x4 e0, e1, n0, n1;
#pragma unroll
    for (int r = 0; r < 4; ++r) {
      e0[r] = __builtin_amdgcn_exp2f(__builtin_fmaf(p[m][0][r], L2E, mx[m])); e1[r] = __builtin_amdgcn_exp2f(__builtin_fmaf(p[m][1][r], L2E, mx[m]));
      n0[r] = __builtin_amdgcn_exp2f(__builtin_fmaf(p[m][0][r], -L2E, nm[m])); n1[r] = __builtin_amdgcn_exp2f(__builtin_fmaf(p[m][1][r], -L2E, nm[m]));
    }
    pos[m] = pack8(e0, e1); neg[m] = pack8(n0, n1);
  }
}
DI u32x4 frag_natural(unsigned x0, unsigned x1, unsigned y0, unsigned y1) {
  auto r0 = __builtin_amdgcn_permlane16_swap(x0, y0, false, false);
  auto r1 = __builtin_amdgcn_permlane16_swap(x1, y1, false, false);
  u32x4 o = {r0[0], r1[0], r0[1], r1[1]};
  return o;
}
DI u32x2 ffn_act(const f32x4& a, const f32x4& b) {
  const f32x4 v = a + b;
  u32x2 r = {cvtpk(v[0] * max2f(v[0], 0.f), v[1] * max2f(v[1], 0.f)), cvtpk(v[2] * max2f(v[2], 0.f), v[3] * max2f(v[3], 0.f))};
  return r;
}
DI f32x4 lds_bias(unsigned addr) { const u32x2 v = *(const LAS u32x2*)(addr); f32x4 r = {lo_f(v[0]), hi_f(v[0]), lo_f(v[1]), hi_f(v[1])}; return r; }
DI void spin_ge(GAS unsigned* w, unsigned want) {
  for (unsigned n = 0; n < (1u << 22); ++n) {
    if ((unsigned)__builtin_amdgcn_readfirstlane(__hip_atomic_load(w, __ATOMIC_RELAXED, __HIP_MEMORY_SCOPE_AGENT)) >= want) return;
    __builtin_amdgcn_s_sleep(1);
  }
}
#define H_AUX 16
template <class R> DI void ffn_pair2(const f32x4 (&t0)[4], const f32x4 (&t1)[4], unsigned bias_addr, const R& rs, unsigned off) {
  const f32x4 b0 = lds_bias(bias_addr), b1 = lds_bias(bias_addr + 32);
#pragma unroll
  for (int m = 0; m < 4; ++m) { const u32x2 x = ffn_act(t0[m], b0), y = ffn_act(t1[m], b1); __builtin_amdgcn_raw_buffer_store_b128(frag_natural(x[0], x[1], y[0], y[1]), rs, off + m * 1024, 0, H_AUX); }
}
template <bool KV> DI void g1_body(char* __restrict__ ws, const u16* __restrict__ wqkv, const u16* __restrict__ wphi, const u16* __restrict__ w1,
                                   const u16* __restrict__ b1, const u16* __restrict__ w2, const u16* __restrict__ wo, int rb, int xc, int tl ST_ARG) {
  constexpr int NT = KV ? 6 : 7;
  const int tid = threadIdx.x, wid = __builtin_amdgcn_readfirstlane(tid >> 6), lane = tid & 63, wm = wid & 3, grp = wid >> 2;
  const int g = lane >> 4, q = (lane & 15) >> 2, p = lane & 3;
  const int hp = head_pair(xc, tl);
  const int fl1 = (tl & 1) * 192, fl2 = KV ? 320 + (tl & 1) * 64 : fl1 + 64, fx = 128 + (tl & 1) * 32;
  f32x4 acc[NT][4];
#pragma unroll
  for (int t = 0; t < NT; ++t)
#pragma unroll
    for (int m = 0; m < 4; ++m) acc[t][m] = (f32x4){0.f, 0.f, 0.f, 0.f};
  Adr ad; Dma d; Pf pf;
  ad.a = wm * 4096 + lane * 16;
  const unsigned lp = (g & 1) * 512 + q * 64 + (g & 1) * 32 + p * 8;
  ad.l0 = 16384 + grp * 2048 + (g >> 1) * 1024 + lp; ad.l1 = ad.l0 ^ 32; ad.lx = (16384 + 12288 + (g >> 1) * 2048 + lp) ^ (grp * 32);
  d.sa = make_srd(ws + XN_OFF, S_ * D_ * 2); d.va0 = d.va1 = lane * 16; d.soa = (rb * 16 + 2 * wid) * 1024; d.stepa = 131072; d.lda = wid * 2048;
  {
    const int line = wid >> 1;
    const bool isq = line == 0 || (KV && line == 1);
    const unsigned pitch = isq ? 6144u : 7168u;
    d.sb = isq ? make_srd_b(wqkv, 1024u * 6144u) : make_srd_b(w1, 1024u * 7168u);
    const int r = lane >> 2, c = (lane & 3) ^ (((r >> 3) & 1) << 1);
    d.vb0 = BIAS + r * pitch + c * 16;
    d.vb1 = d.vb0 - 2048 + (line < 3 ? 64u : 16u * pitch);
    const int col = isq ? (KV ? line + 1 : 0) * 1024 + hp * 64 : xc * 448 + (line == 1 ? fl1 : line == 2 ? fl2 : fx);
    d.sob = col * 2 + (wid & 1) * 16 * pitch; d.stepb = 32 * pitch; d.ldb = 16384 + line * 4096 + (wid & 1) * 1024;
  }
  pf.i = -1;
  if (wid == 7) {
    dma16<0>(make_srd(b1, F_ * 2), lane * 16, xc * 896, G1_EXTRA + 4096);
    const i32x4 sp = make_srd(wphi, H_ * 2048);
    dma16<0>(sp, lane * 16, hp * 4096, G1_EXTRA); dma16<1024>(sp, lane * 16, hp * 4096, G1_EXTRA); dma16<2048>(sp, lane * 16, hp * 4096, G1_EXTRA); dma16<3072>(sp, lane * 16, hp * 4096, G1_EXTRA);
    pf.i = rb * 4 + tl;
  }
  pf.sq = make_srd(wqkv, 1024u * 6144u); pf.s1 = make_srd(w1, 1024u * 7168u);
  pf.vq = lane < 48 ? (lane / 6) * 6144u + qkv_col(xc, lane % 6) : OOB; pf.v1 = lane < 56 ? (lane / 7) * 7168u + xc * 896 + (lane % 7) * 128 : OOB;
  pf.s2 = make_srd(w2, F_ * 2048u); pf.so = make_srd(wo, 1024u * 2048u); pf.o2 = xc * 144;
  if (wid < NT) g1_mainloop<NT, 2, KV>(acc, ad, d, pf, lane ST_PASS);
  else g1_mainloop<NT, 0, KV>(acc, ad, d, pf, lane ST_PASS);
  ST_MARK(2);
  bar();
  const int hd = hp * 2 + grp;
  const f32x4 zero = {0.f, 0.f, 0.f, 0.f};
  const int G = ((g & 1) << 1) | (g >> 1);
  const auto ra2 = __builtin_amdgcn_make_buffer_rsrc(ws + A2_OFF, 0, (unsigned)A2_BYTES, 0x00020000);
  const unsigned a2r = (rb * KS2 * 16 + wm * 4) * 1024 + (lane & 15) * 16, a2b = a2r + xc * 14 * 16384;
  const unsigned bb = G1_EXTRA + 4096 + g * 8;
  if constexpr (!KV) {
    ffn_pair2(acc[2], acc[3], bb + (fl1 + grp * 32) * 2, ra2, a2b + ((fl1 >> 5) + grp) * 16384 + G * 256);
    ffn_pair2(acc[4], acc[5], bb + (fl2 + grp * 32) * 2, ra2, a2b + ((fl2 >> 5) + grp) * 16384 + G * 256);
    const f32x4 b0 = lds_bias(bb + (fx + grp * 16) * 2);
#pragma unroll
    for (int m = 0; m < 4; ++m) __builtin_amdgcn_raw_buffer_store_b64(ffn_act(acc[NT - 1][m], b0), ra2, a2b + (fx >> 5) * 16384 + (grp * 2 + (g >> 1)) * 256 + (g & 1) * 8 + m * 1024, 0, H_AUX);
    SB();
  }
  bf16x8 pos[4], neg[4];
  {
    bf16x8 wp[2];
#pragma unroll
    for (int ft = 0; ft < 2; ++ft) { const unsigned a = G1_EXTRA + grp * 2048 + (4 * g + q) * 64 + ft * 32 + p * 8; wp[ft] = cat(lds_tr(a), lds_tr(a + 1024)); }
    f32x4 pp[4][2];
#pragma unroll
    for (int m = 0; m < 4; ++m) { const bf16x8 qf = pack8(acc[0][m], acc[1][m]); pp[m][0] = MFMA(wp[0], qf, zero); pp[m][1] = MFMA(wp[1], qf, zero); }
    dual_exp(pp, pos, neg);
  }
  const u32x4 onesu = {0x3f803f80u, 0x3f803f80u, 0x3f803f80u, 0x3f803f80u};
  const bf16x8 ones = __builtin_bit_cast(bf16x8, onesu);
  GAS unsigned* cnt = (GAS unsigned*)(ws + FLG_OFF) + hp * 64;
  const auto rkv = __builtin_amdgcn_make_buffer_rsrc(ws + KVP_OFF, 0, H_ * 8 * KVP_STRIDE * 4, 0x00020000);
  const int t2 = tid & 255;
  if constexpr (KV) {
    bf16x8 idm[2];
#pragma unroll
    for (int ft = 0; ft < 2; ++ft) {
      const unsigned one = (g == q) ? ((p & 1) ? 0x3f800000u : 0x00003f80u) : 0u;
      u32x4 iv = {0u, 0u, 0u, 0u};
      iv[ft * 2] = (p >> 1) == 0 ? one : 0u; iv[ft * 2 + 1] = (p >> 1) == 1 ? one : 0u;
      idm[ft] = __builtin_bit_cast(bf16x8, iv);
    }
    f32x4 kv[4][2], zz[4];
#pragma unroll
    for (int f4 = 0; f4 < 4; ++f4) { kv[f4][0] = zero; kv[f4][1] = zero; zz[f4] = zero; }
#pragma unroll
    for (int u = 0; u < 2; ++u) {
      f32x4 tr[2][4];
#pragma unroll
      for (int mm = 0; mm < 2; ++mm) {
        const int m = 2 * u + mm;
        const f32x4 sp = MFMA(pos[m], ones, zero), sn = MFMA(neg[m], ones, zero);
        f32x4 rp, rn;
#pragma unroll
        for (int r = 0; r < 4; ++r) { rp[r] = __builtin_amdgcn_rcpf(sp[r]); rn[r] = __builtin_amdgcn_rcpf(sn[r]); }
        tr[mm][0] = MFMA(pos[m], idm[0], zero) * rp; tr[mm][1] = MFMA(pos[m], idm[1], zero) * rp;
        tr[mm][2] = MFMA(neg[m], idm[0], zero) * rn; tr[mm][3] = MFMA(neg[m], idm[1], zero) * rn;
      }
      const bf16x8 v0 = pack8(acc[2][2 * u], acc[2][2 * u + 1]), v1 = pack8(acc[3][2 * u], acc[3][2 * u + 1]);
#pragma unroll
      for (int f4 = 0; f4 < 4; ++f4) {
        const bf16x8 ph = pack8(tr[0][f4], tr[1][f4]);
        kv[f4][0] = MFMA(ph, v0, kv[f4][0]); kv[f4][1] = MFMA(ph, v1, kv[f4][1]); zz[f4] = MFMA(ph, ones, zz[f4]);
      }
    }
    const unsigned red = wid * (KVP_STRIDE * 4);
#pragma unroll
    for (int f4 = 0; f4 < 4; ++f4) {
#pragma unroll
      for (int t = 0; t < 2; ++t) *(LAS f32x4*)(red + (((f4 >> 1) * 2 + t) * 64 + lane) * 32 + (f4 & 1) * 16) = kv[f4][t];
      if ((lane & 15) == 0) *(LAS f32x4*)(red + 8192 + ((f4 >> 1) * 4 + g) * 32 + (f4 & 1) * 16) = zz[f4];
    }
    wait_lgkm0();
    bar();
    const unsigned src = grp * 4 * (KVP_STRIDE * 4);
#pragma unroll
    for (int i = 0; i < 3; ++i) {
      const int e = (t2 + 256 * i) * 4;
      if (i < 2 || e < KVP_STRIDE) {
        f32x4 sv = *(const LAS f32x4*)(src + e * 4);
#pragma unroll
        for (int w = 1; w < 4; ++w) sv += *(const LAS f32x4*)(src + w * (KVP_STRIDE * 4) + e * 4);
        __builtin_amdgcn_raw_buffer_store_b128(__builtin_bit_cast(u32x4, sv), rkv, ((hd * 8 + rb) * KVP_STRIDE + e) * 4, 0, 16);
      }
    }
    ST_MARK(3);
    wait_vm<0>();
    bar();
    if (tid == 0) __hip_atomic_fetch_add(cnt, 1u, __ATOMIC_RELAXED, __HIP_MEMORY_SCOPE_AGENT);
    ST_MARK(4);
    ffn_pair2(acc[4], acc[5], bb + (fl2 + grp * 32) * 2, ra2, a2b + ((fl2 >> 5) + grp) * 16384 + G * 256);
  } else {
    float rp[4], rn[4];
#pragma unroll
    for (int m = 0; m < 4; ++m) { rp[m] = __builtin_amdgcn_rcpf(MFMA(ones, pos[m], zero)[0]); rn[m] = __builtin_amdgcn_rcpf(MFMA(ones, neg[m], zero)[0]); }
    asm volatile("" : "+v"(rp[0]), "+v"(rp[1]), "+v"(rp[2]), "+v"(rp[3]), "+v"(rn[0]), "+v"(rn[1]), "+v"(rn[2]), "+v"(rn[3]));
    ST_MARK(3);
    if (wid == 0) spin_ge(cnt, 8u);
    bar();
    __builtin_amdgcn_fence(__ATOMIC_ACQUIRE, "wavefront");
    ST_MARK(4);
    f32x4 s0, s1;
    {
      f32x4 pv[8][2];
#pragma unroll
      for (int b = 0; b < 8; ++b) {
        pv[b][0] = __builtin_bit_cast(f32x4, __builtin_amdgcn_raw_buffer_load_b128(rkv, ((hd * 8 + b) * KVP_STRIDE + t2 * 4) * 4, 0, 16));
        pv[b][1] = __builtin_bit_cast(f32x4, __builtin_amdgcn_raw_buffer_load_b128(rkv, ((hd * 8 + b) * KVP_STRIDE + 1024 + t2 * 4) * 4, 0, 16));
      }
      if (t2 < 16) {
        f32x4 pz[8];
#pragma unroll
        for (int b = 0; b < 8; ++b) pz[b] = __builtin_bit_cast(f32x4, __builtin_amdgcn_raw_buffer_load_b128(rkv, ((hd * 8 + b) * KVP_STRIDE + 2048 + t2 * 4) * 4, 0, 16));
        const f32x4 z = ((pz[0] + pz[1]) + (pz[2] + pz[3])) + ((pz[4] + pz[5]) + (pz[6] + pz[7]));
        *(LAS u32x2*)(grp * 8192 + 4096 + t2 * 8) = (u32x2){cvtpk(z[0], z[1]), cvtpk(z[2], z[3])};
      }
      s0 = ((pv[0][0] + pv[1][0]) + (pv[2][0] + pv[3][0])) + ((pv[4][0] + pv[5][0]) + (pv[6][0] + pv[7][0]));
      s1 = ((pv[0][1] + pv[1][1]) + (pv[2][1] + pv[3][1])) + ((pv[4][1] + pv[5][1]) + (pv[6][1] + pv[7][1]));
    }
    *(LAS u32x2*)(grp * 8192 + t2 * 8) = (u32x2){cvtpk(s0[0], s0[1]), cvtpk(s0[2], s0[3])};
    *(LAS u32x2*)(grp * 8192 + 2048 + t2 * 8) = (u32x2){cvtpk(s1[0], s1[1]), cvtpk(s1[2], s1[3])};
    wait_lgkm0();
    bar();
    bf16x8 kv[2][2], zf[2];
#pragma unroll
    for (int pn = 0; pn < 2; ++pn) {
      kv[pn][0] = lds_b128(grp * 8192 + (pn * 2) * 1024 + lane * 16); kv[pn][1] = lds_b128(grp * 8192 + (pn * 2 + 1) * 1024 + lane * 16);
      zf[pn] = lds_b128(grp * 8192 + 4096 + (pn * 4 + g) * 16);
    }
#pragma unroll
    for (int m = 0; m < 4; ++m) {
      const f32x4 np0 = MFMA(kv[0][0], pos[m], zero), np1 = MFMA(kv[0][1], pos[m], zero), nn0 = MFMA(kv[1][0], neg[m], zero), nn1 = MFMA(kv[1][1], neg[m], zero);
      const f32x4 dp = MFMA(zf[0], pos[m], zero), dn = MFMA(zf[1], neg[m], zero);
      const float inv = __builtin_amdgcn_rcpf(dp[0] * rp[m] + dn[0] * rn[m] + 1e-6f);
      const float cp = rp[m] * inv, cn = rn[m] * inv;
      const f32x4 o0 = np0 * cp + nn0 * cn, o1 = np1 * cp + nn1 * cn;
      __builtin_amdgcn_raw_buffer_store_b128(frag_natural(cvtpk(o0[0], o0[1]), cvtpk(o0[2], o0[3]), cvtpk(o1[0], o1[1]), cvtpk(o1[2], o1[3])), ra2, a2r + (112 + hd) * 16384 + G * 256 + m * 1024, 0, H_AUX);
    }
  }
  ST_MARK(5);
  ST_FIN(6);
}
__global__ __launch_bounds__(512, 2) void k_gemm1(char* __restrict__ ws, const u16* __restrict__ wqkv, const u16* __restrict__ wphi, const u16* __restrict__ w1,
                                                  const u16* __restrict__ b1, const u16* __restrict__ w2, const u16* __restrict__ wo) {
  ST_INIT(ws, 1);
  const int xc = blockIdx.x & 7, idx = blockIdx.x >> 3, rb = idx >> 2, tl = idx & 3;
  if (tl < 2) g1_body<true>(ws, wqkv, wphi, w1, b1, w2, wo, rb, xc, tl ST_PASS);
  else g1_body<false>(ws, wqkv, wphi, w1, b1, w2, wo, rb, xc, tl ST_PASS);
}

constexpr int G2_SLOT = 24576, G2_LDS = G2_SLOT * 6;
__global__ __launch_bounds__(512, 2) void k_gemm2(char* __restrict__ ws, const u16* __restrict__ w2, const u16* __restrict__ wo, const u16* __restrict__ x,
                                                  const u16* __restrict__ b2, u16* __restrict__ out) {
  ST_INIT(ws, 3);
  const int tid = threadIdx.x, wid = __builtin_amdgcn_readfirstlane(tid >> 6), lane = tid & 63;
  const int wm = wid & 3, wn = wid >> 2;
  const int xc = blockIdx.x & 7, idx = blockIdx.x >> 3;
  const int kq = xc >> 1, rb = (xc & 1) * 4 + (idx & 3), ct = idx >> 2;
  const int g = lane >> 4, q = (lane & 15) >> 2, p = lane & 3;
  Adr ad0; Dma d;
  ad0.a = wm * 4096 + lane * 16;
  ad0.l0 = 16384 + wn * 4096 + (g >> 1) * 1024 + (g & 1) * 512 + q * 64 + (g & 1) * 32 + p * 8; ad0.l1 = ad0.l0 ^ 32; ad0.lx = 0;
  d.sa = make_srd_b(ws + A2_OFF, (rb * KS2 + kq * 36 + 36) * 16384);
  d.va0 = BIAS + lane * 16 + (((wid & 1) * 2 + kq) & 3) * 1024; d.va1 = BIAS + lane * 16 + (((wid & 1) * 2 + 1 + kq) & 3) * 1024 - 1024;
  d.soa = ((rb * KS2 + kq * 36) * 16 + (wid >> 1) * 4) * 1024; d.stepa = 16384; d.lda = wid * 2048;
  d.sb = make_srd(w2, (kq == 3 ? F_ : (kq + 1) * 1152) * 2048u);
  { const int r = lane >> 2, c = (lane & 3) ^ (((r >> 3) & 1) << 1); d.vb0 = d.vb1 = r * 2048 + c * 16; }
  const unsigned sob0 = (wid >> 2) * 16 * 2048 + (ct * 128 + (wid & 3) * 32) * 2;
  d.sob = sob0 + kq * 1152 * 2048; d.stepb = 32 * 2048; d.ldb = 16384 + (wid & 3) * 2048 + (wid >> 2) * 1024;
  k_dma_all<1, 0>(d); k_dma_all<1, G2_SLOT>(d); k_dma_all<1, 2 * G2_SLOT>(d); k_dma_all<1, 3 * G2_SLOT>(d);
  if (kq == 3) { d.sb = make_srd(wo, 1024u * 2048u); d.sob = sob0; }
  k_dma_all<1, 4 * G2_SLOT>(d); k_dma_all<1, 5 * G2_SLOT>(d);
  f32x4 acc[4][4];
#pragma unroll
  for (int t = 0; t < 4; ++t)
#pragma unroll
    for (int m = 0; m < 4; ++m) acc[t][m] = (f32x4){0.f, 0.f, 0.f, 0.f};
  {
    const Adr ad1 = adr_at(ad0, 2 * G2_SLOT), ad2 = adr_at(ad0, 4 * G2_SLOT);
    Frag<4> f0, f1;
    wait_vm<15>(); bar();
    k_read_all<4, 2048, 0>(f0, ad0, std::make_integer_sequence<int, 12>{});
    wait_lgkm0(); wait_vm<12>();
    ST_MARK(1);
    for (int it = 0; it < 6; ++it) {
      k_step<4, 1, false, 2048, G2_SLOT, 0, 4>(acc, f0, f1, ad0, d);
      k_step<4, 1, false, 2048, 0, G2_SLOT, 4>(acc, f1, f0, ad1, d);
      k_step<4, 1, false, 2048, G2_SLOT, 2 * G2_SLOT, 4>(acc, f0, f1, ad1, d);
      k_step<4, 1, false, 2048, 0, 3 * G2_SLOT, 4>(acc, f1, f0, ad2, d);
      k_step<4, 1, false, 2048, G2_SLOT, 4 * G2_SLOT, 4>(acc, f0, f1, ad2, d);
      k_step<4, 1, false, 2048, 0, 5 * G2_SLOT, 4>(acc, f1, f0, ad0, d);
    }
    __builtin_amdgcn_s_setprio(0);
    acc_fence();
  }
  ST_MARK(2);
  GAS unsigned* flags = (GAS unsigned*)(ws + FLG_OFF) + 16 * 64;
  const auto rx = __builtin_amdgcn_make_buffer_rsrc(ws + XCH_OFF, 0, 256u * 49152u, 0x00020000);
#pragma unroll
  for (int i = 1; i < 4; ++i)
#pragma unroll
    for (int h = 0; h < 2; ++h)
      __builtin_amdgcn_raw_buffer_store_b128(__builtin_bit_cast(u32x4, pack8(acc[2 * h][i], acc[2 * h + 1][i])), rx, blockIdx.x * 49152 + (((i - 1) * 8 + wid) * 2 + h) * 1024 + lane * 16, 0, 16);
  const int G = ((g & 1) << 1) | (g >> 1);
  const size_t go = (size_t)(rb * 256 + wm * 64 + kq * 16 + (lane & 15)) * D_ + ct * 128 + wn * 64 + G * 8;
  u32x4 xv[2], bv[2];
#pragma unroll
  for (int h = 0; h < 2; ++h) { xv[h] = *(const u32x4*)(x + go + h * 32); bv[h] = *(const u32x4*)(b2 + ct * 128 + wn * 64 + G * 8 + h * 32); }
  wait_vm<0>();
  bar();
  ST_MARK(3);
  if (wid == 0) {
    if (lane == 0) __hip_atomic_store(flags + blockIdx.x * 64, 1u, __ATOMIC_RELAXED, __HIP_MEMORY_SCOPE_AGENT);
    GAS unsigned* fp = flags + ((blockIdx.x & ~6) | ((lane & 3) << 1)) * 64;
    for (unsigned n = 0; n < (1u << 22); ++n) {
      if (__all(__hip_atomic_load(fp, __ATOMIC_RELAXED, __HIP_MEMORY_SCOPE_AGENT) != 0u)) break;
      __builtin_amdgcn_s_sleep(1);
    }
  }
  bar();
  __builtin_amdgcn_fence(__ATOMIC_ACQUIRE, "wavefront");
  ST_MARK(4);
  u32x4 pr[3][2];
#pragma unroll
  for (int j = 1; j < 4; ++j) {
    const unsigned src = (blockIdx.x & ~6) | (((kq + j) & 3) << 1);
#pragma unroll
    for (int h = 0; h < 2; ++h) pr[j - 1][h] = __builtin_amdgcn_raw_buffer_load_b128(rx, src * 49152 + (((3 - j) * 8 + wid) * 2 + h) * 1024 + lane * 16, 0, 16);
  }
#pragma unroll
  for (int h = 0; h < 2; ++h) {
    f32x4 v0 = acc[2 * h][0], v1 = acc[2 * h + 1][0];
#pragma unroll
    for (int j = 0; j < 3; ++j) {
      v0 += (f32x4){lo_f(pr[j][h][0]), hi_f(pr[j][h][0]), lo_f(pr[j][h][1]), hi_f(pr[j][h][1])};
      v1 += (f32x4){lo_f(pr[j][h][2]), hi_f(pr[j][h][2]), lo_f(pr[j][h][3]), hi_f(pr[j][h][3])};
    }
#pragma unroll
    for (int r = 0; r < 4; ++r) { float a = v0[r], b = v1[r]; swap16(a, b); v0[r] = a; v1[r] = b; }
    u32x4 o;
    o[0] = cvtpk(v0[0] + lo_f(xv[h][0]) + lo_f(bv[h][0]), v0[1] + hi_f(xv[h][0]) + hi_f(bv[h][0]));
    o[1] = cvtpk(v0[2] + lo_f(xv[h][1]) + lo_f(bv[h][1]), v0[3] + hi_f(xv[h][1]) + hi_f(bv[h][1]));
    o[2] = cvtpk(v1[0] + lo_f(xv[h][2]) + lo_f(bv[h][2]), v1[1] + hi_f(xv[h][2]) + hi_f(bv[h][2]));
    o[3] = cvtpk(v1[2] + lo_f(xv[h][3]) + lo_f(bv[h][3]), v1[3] + hi_f(xv[h][3]) + hi_f(bv[h][3]));
    *(u32x4*)(out + go + h * 32) = o;
  }
  ST_MARK(5);
  ST_FIN(6);
}

extern "C" void kernel_launch(void* const* d_in, const int* in_sizes, int n_in, void* d_out, int out_size, void* d_ws, size_t ws_size, hipStream_t stream) {
  const u16* x = (const u16*)d_in[0]; const u16* wqkv = (const u16*)d_in[1]; const u16* wphi = (const u16*)d_in[2]; const u16* wo = (const u16*)d_in[3];
  const u16* w1 = (const u16*)d_in[5]; const u16* b1 = (const u16*)d_in[6]; const u16* w2 = (const u16*)d_in[7]; const u16* b2 = (const u16*)d_in[8];
  char* ws = (char*)d_ws;
  for (int i = 0; i < n_in; ++i) fprintf(stderr, "in[%d]=%p ", i, d_in[i]); fprintf(stderr, "out=%p ws=%p\n", d_out, d_ws);
  (void)hipFuncSetAttribute((const void*)k_gemm1, hipFuncAttributeMaxDynamicSharedMemorySize, G1_LDS);
  (void)hipFuncSetAttribute((const void*)k_gemm2, hipFuncAttributeMaxDynamicSharedMemorySize, G2_LDS);
  k_ln<<<256, 256, 0, stream>>>(x, ws, wqkv, w1, wphi, b1);
  k_gemm1<<<256, 512, G1_LDS, stream>>>(ws, wqkv, wphi, w1, b1, w2, wo);
  k_gemm2<<<256, 512, G2_LDS, stream>>>(ws, w2, wo, x, b2, (u16*)d_out);
}
```

```cpp
#define STAMP
#define FIRSTK 0
#define QSEL -1
#include <hip/hip_runtime.h>
#include <utility>

typedef unsigned short u16;
typedef __bf16 bf16x8 __attribute__((ext_vector_type(8)));
typedef __bf16 bf16x2 __attribute__((ext_vector_type(2)));
typedef short s16x4 __attribute__((ext_vector_type(4)));
typedef float f32x2 __attribute__((ext_vector_type(2)));
typedef float f32x4 __attribute__((ext_vector_type(4)));
typedef unsigned u32x2 __attribute__((ext_vector_type(2)));
typedef unsigned u32x4 __attribute__((ext_vector_type(4)));
typedef int i32x4 __attribute__((ext_vector_type(4)));
#define LAS __attribute__((address_space(3)))
#define GAS __attribute__((address_space(1)))
#define DI __device__ __forceinline__
#define SB() __builtin_amdgcn_sched_barrier(0)
#define MFMA(a, b, c) __builtin_amdgcn_mfma_f32_16x16x32_bf16((a), (b), (c), 0, 0, 0)

constexpr int S_ = 2048, D_ = 1024, H_ = 32, F_ = 3584;
constexpr int KS2 = 144;
constexpr size_t XN_OFF = 0;
constexpr size_t A2_OFF = 4u << 20;
constexpr size_t A2_BYTES = (size_t)S_ * 4608 * 2;
constexpr size_t KVP_OFF = 32u << 20;
constexpr int KVP_STRIDE = 2112;
constexpr size_t XCH_OFF = 36u << 20;
constexpr size_t FLG_OFF = 52u << 20;

DI unsigned cvtpk(float lo, float hi) { f32x2 v = {lo, hi}; bf16x2 b = __builtin_convertvector(v, bf16x2); return __builtin_bit_cast(unsigned, b); }
DI float rbf(float x) { return __uint_as_float(cvtpk(x, 0.f) << 16); }
DI float lo_f(unsigned p) { return __uint_as_float(p << 16); }
DI float hi_f(unsigned p) { return __uint_as_float(p & 0xffff0000u); }
DI bf16x8 pack8(const f32x4& a, const f32x4& b) { u32x4 r = {cvtpk(a[0], a[1]), cvtpk(a[2], a[3]), cvtpk(b[0], b[1]), cvtpk(b[2], b[3])}; return __builtin_bit_cast(bf16x8, r); }
DI i32x4 make_srd(const void* p, unsigned bytes) { unsigned long long a = (unsigned long long)p; i32x4 r; r[0] = (int)(unsigned)a; r[1] = (int)((unsigned)(a >> 32) & 0xffffu); r[2] = (int)bytes; r[3] = 0x00020000; return r; }

template <int OFF> DI void dma16(const i32x4& srd, unsigned voff, unsigned soff, unsigned lds) {
  asm volatile("s_mov_b32 m0, %3\n\ts_nop 0\n\tbuffer_load_dwordx4 %0, %1, %2 offen offset:%4 lds" ::"v"(voff), "s"(srd), "s"(soff), "s"(lds), "i"(OFF) : "memory");
}
template <int IMM> DI void set_m0(unsigned base) { asm volatile("s_add_u32 m0, %0, %1" ::"s"(base), "i"(IMM) : "scc"); }
template <int OFF> DI void dma_ld(const i32x4& srd, unsigned voff, unsigned soff) {
  asm volatile("buffer_load_dwordx4 %0, %1, %2 offen offset:%3 lds" ::"v"(voff), "s"(srd), "s"(soff), "i"(OFF) : "memory");
}
template <int OFF> DI void dma_ld_nt(const i32x4& srd, unsigned voff, unsigned soff) {
  asm volatile("buffer_load_dwordx4 %0, %1, %2 offen offset:%3 nt lds" ::"v"(voff), "s"(srd), "s"(soff), "i"(OFF) : "memory");
}
DI void nop1() { asm volatile("s_nop 0"); }
DI void touch(const i32x4& srd, unsigned voff, unsigned soff, unsigned lds) {
  asm volatile("s_mov_b32 m0, %3\n\ts_nop 0\n\tbuffer_load_dword %0, %1, %2 offen lds" ::"v"(voff), "s"(srd), "s"(soff), "s"(lds) : "memory");
}
constexpr unsigned OOB = 0x7fffffffu;
template <int N> DI void wait_vm() { asm volatile("s_waitcnt vmcnt(%0)" ::"i"(N) : "memory"); }
DI void wait_lgkm0() { __builtin_amdgcn_s_waitcnt(0xC07F); }
DI void bar() { asm volatile("" ::: "memory"); __builtin_amdgcn_s_barrier(); asm volatile("" ::: "memory"); }
DI bf16x8 lds_b128(unsigned addr) { return *(const LAS bf16x8*)(addr); }
DI s16x4 lds_tr(unsigned addr) { return __builtin_amdgcn_ds_read_tr16_b64_v4i16((LAS s16x4*)(addr)); }
DI bf16x8 cat(const s16x4& a, const s16x4& b) { return __builtin_bit_cast(bf16x8, __builtin_shufflevector(a, b, 0, 1, 2, 3, 4, 5, 6, 7)); }

constexpr size_t STAMP_OFF = 64u << 20; constexpr int NSTAMPK = 4;
struct Stamper { unsigned long long* p; unsigned long long t0, t1, t2, t3, t4, t5, t6, t7, r0; unsigned f0, f1, f2; unsigned long long ra, rb;
  __device__ __forceinline__ void init(void* ws, int k) { p = (unsigned long long*)((char*)ws + STAMP_OFF) + ((size_t)(k * 256 + blockIdx.x) * 8 + (threadIdx.x >> 6)) * 16; t0 = t1 = t2 = t3 = t4 = t5 = t6 = t7 = 0; f0 = f1 = f2 = 0; ra = rb = 0; r0 = __builtin_amdgcn_s_memrealtime(); mark<0>(); }
  template <int I> __device__ __forceinline__ void mark() { unsigned long long t; __builtin_amdgcn_sched_barrier(0); asm volatile("s_memtime %0\n\ts_waitcnt lgkmcnt(0)" : "=s"(t) :: "memory"); __builtin_amdgcn_sched_barrier(0);
    if constexpr (I == 1) { asm volatile("s_memrealtime %0\n\ts_waitcnt lgkmcnt(0)" : "=s"(ra) :: "memory"); } if constexpr (I == 2) { asm volatile("s_memrealtime %0\n\ts_waitcnt lgkmcnt(0)" : "=s"(rb) :: "memory"); }
    if constexpr (I == 0) t0 = t; if constexpr (I == 1) t1 = t; if constexpr (I == 2) t2 = t; if constexpr (I == 3) t3 = t; if constexpr (I == 4) t4 = t; if constexpr (I == 5) t5 = t; if constexpr (I == 6) t6 = t; if constexpr (I == 7) t7 = t; }
  template <int I> __device__ __forceinline__ void fin() { mark<I>(); unsigned long long r = __builtin_amdgcn_s_memrealtime(); if ((threadIdx.x & 63) == 0) { p[14] = r; p[15] = r0; p[0] = t0; p[1] = t1; p[2] = t2; p[3] = t3; p[4] = t4; p[5] = t5; p[6] = t6; p[7] = t7; p[8] = 0; p[9] = ra; p[13] = rb; p[10] = f0; p[11] = f1; p[12] = f2; } }
};
#define ST_ARG , Stamper& st_
#define ST_PASS , st_
#define ST_INIT(ws, k) Stamper st_; st_.init((void*)(ws), k)
#define ST_MARK(i) st_.template mark<i>()
#define ST_FIN(i) do { wait_vm<0>(); st_.template fin<i>(); } while (0)

DI int head_pair(int xc, int tl) { return ((7 * xc + (tl < 2 ? 7 : 3)) & 7) + 8 * (tl & 1); }
DI unsigned qkv_col(int xc, int c) { return (c >> 1) * 2048 + head_pair(xc, (c < 2 ? 2 : 0) + (c & 1)) * 128; }
DI unsigned qkv_lines(int xc, int i, int lane) { const int r = lane / 6, c = lane % 6; return lane < 48 ? (unsigned)((r * 32 + i) * 6144) + qkv_col(xc, c) : OOB; }
DI unsigned w1_lines(int xc, int i, int lane) { const int r = lane / 7, c = lane % 7; return lane < 56 ? (unsigned)((r * 32 + i) * 7168 + xc * 896 + c * 128) : OOB; }
#define G1_PF0 1
__global__ __launch_bounds__(256) void k_ln(const u16* __restrict__ x, char* __restrict__ ws, const u16* __restrict__ wqkv, const u16* __restrict__ w1,
                                            const u16* __restrict__ wphi, const u16* __restrict__ b1) {
  __shared__ float red[2][4][8];
  __shared__ unsigned scratch[64];
  const int t = threadIdx.x, r = t & 7, c0 = t >> 3, w = t >> 6;
  ST_INIT(ws, 0);
  const int row = blockIdx.x * 8 + r;
  u32x4 v[4];
#pragma unroll
  for (int i = 0; i < 4; ++i) v[i] = *(const u32x4*)(x + (size_t)row * D_ + (c0 + 32 * i) * 8);
  {
    const int xc = blockIdx.x & 7, idx = blockIdx.x >> 3, lane = t & 63, wv = __builtin_amdgcn_readfirstlane(w);
    const unsigned sc = (unsigned)(size_t)(LAS unsigned*)scratch;
    if (wv == 0) { for (int j = 0; j < G1_PF0; ++j) touch(make_srd(wqkv, 1024u * 6144u), qkv_lines(xc, idx, lane), j * 256 * 6144, sc); }
    else if (wv == 1) { for (int j = 0; j < G1_PF0; ++j) touch(make_srd(w1, 1024u * 7168u), w1_lines(xc, idx, lane), j * 256 * 7168, sc); }
    else if (wv == 2 && idx < 4) touch(make_srd(wphi, H_ * 2048), lane < 32 ? lane * 128u : OOB, head_pair(xc, idx) * 4096, sc);
    else if (wv == 3 && idx == 0) touch(make_srd(b1, F_ * 2), lane < 7 ? lane * 128u : OOB, xc * 896, sc);
  }
  if (t == 0) ((unsigned*)(ws + FLG_OFF))[(16 + blockIdx.x) * 64] = 0u;
  if (t == 1 && blockIdx.x < 16) ((unsigned*)(ws + FLG_OFF))[blockIdx.x * 64] = 0u;
  float s = 0.f;
#pragma unroll
  for (int i = 0; i < 4; ++i)
#pragma unroll
    for (int j = 0; j < 4; ++j) s += lo_f(v[i][j]) + hi_f(v[i][j]);
  s += __shfl_xor(s, 8); s += __shfl_xor(s, 16); s += __shfl_xor(s, 32);
  if ((t & 63) < 8) red[0][w][r] = s;
  __syncthreads();
  const float mean = (red[0][0][r] + red[0][1][r] + red[0][2][r] + red[0][3][r]) * (1.f / D_);
  float q = 0.f;
#pragma unroll
  for (int i = 0; i < 4; ++i)
#pragma unroll
    for (int j = 0; j < 4; ++j) { float a = lo_f(v[i][j]) - mean, b = hi_f(v[i][j]) - mean; q += a * a + b * b; }
  q += __shfl_xor(q, 8); q += __shfl_xor(q, 16); q += __shfl_xor(q, 32);
  if ((t & 63) < 8) red[1][w][r] = q;
  __syncthreads();
  const float var = (red[1][0][r] + red[1][1][r] + red[1][2][r] + red[1][3][r]) * (1.f / D_);
  const float mb = rbf(mean);
  const float den = rbf(sqrtf(rbf(rbf(var) + 1e-5f)));
  const float inv = 1.0f / den;
  const int rb = row >> 8, mt = (row >> 4) & 15, rr = row & 15;
#pragma unroll
  for (int i = 0; i < 4; ++i) {
    u32x4 o;
#pragma unroll
    for (int j = 0; j < 4; ++j) {
      unsigned d = cvtpk(lo_f(v[i][j]) - mb, hi_f(v[i][j]) - mb);
      o[j] = cvtpk(lo_f(d) * inv, hi_f(d) * inv);
    }
    const int c = c0 + 32 * i, ks = c >> 2, g = c & 3;
    *(u32x4*)(ws + XN_OFF + ((size_t)((ks * 8 + rb) * 16 + mt) * 64 + g * 16 + rr) * 16) = o;
  }
  ST_MARK(1);
  ST_FIN(2);
}

DI void mfma_acc(f32x4& acc, const bf16x8& a, const bf16x8& b) { asm volatile("v_mfma_f32_16x16x32_bf16 %0, %1, %2, %0" : "+v"(acc) : "v"(a), "v"(b)); }
DI void acc_fence() { SB(); asm volatile("s_nop 7\n\ts_nop 7"); SB(); }
template <int NT> struct Frag { bf16x8 a[4]; s16x4 b[NT][2]; };
struct Adr { unsigned a, l0, l1, lx; };
struct Dma { i32x4 sa, sb; unsigned va0, va1, vb0, vb1, soa, sob, stepa, stepb, lda, ldb; };
#define NT1 0
#define NT2 0
#define PF2LATE 0
#define KTAIL 3
template <int NT, int LSTR, int RO, int R> DI void k_read(Frag<NT>& f, const Adr& ad) {
  if constexpr (R < 2) f.b[0][R] = lds_tr(ad.l0 + RO + R * 256);
  else if constexpr (R < 6) f.a[R - 2] = lds_b128(ad.a + RO + (R - 2) * 1024);
  else {
    constexpr int t = (R - 4) >> 1, e = (R - 4) & 1;
    if constexpr (t < 6) f.b[t][e] = lds_tr(((t & 1) ? ad.l1 : ad.l0) + RO + (t >> 1) * LSTR + e * 256);
    else f.b[t][e] = lds_tr(ad.lx + RO + e * 256);
  }
}
template <int NT, int LSTR, int RO, int... R> DI void k_read_all(Frag<NT>& f, const Adr& ad, std::integer_sequence<int, R...>) { (k_read<NT, LSTR, RO, R>(f, ad), ...); }
template <int NB, int WO, int P> DI void k_dma(Dma& d) {
  if constexpr (P == 0) dma_ld<0>(d.sa, d.va0, d.soa);
  if constexpr (P == 1) { dma_ld<1024>(d.sa, d.va1, d.soa); d.soa += d.stepa; if constexpr (NB > 0) set_m0<WO>(d.ldb); }
  if constexpr (P == 2) { if constexpr ((NB == 1 && NT2) || (NB == 2 && NT1)) dma_ld_nt<0>(d.sb, d.vb0, d.sob); else dma_ld<0>(d.sb, d.vb0, d.sob); if constexpr (NB == 1) d.sob += d.stepb; }
  if constexpr (P == 3) { if constexpr (NT1) dma_ld_nt<2048>(d.sb, d.vb1, d.sob); else dma_ld<2048>(d.sb, d.vb1, d.sob); d.sob += d.stepb; }
}
template <int NB, int WO> DI void k_dma_all(Dma& d) {
  set_m0<WO>(d.lda); nop1(); k_dma<NB, WO, 0>(d); k_dma<NB, WO, 1>(d);
  if constexpr (NB > 0) { nop1(); k_dma<NB, WO, 2>(d); }
  if constexpr (NB > 1) k_dma<NB, WO, 3>(d);
}
template <int NT, int NB, int LSTR, int RO, int WO, int J> DI void k_memop(Frag<NT>& n, const Adr& ad, Dma& d) {
  constexpr int ND = 2 + NB, TOT = 4 + 2 * NT + ND, before = (J * ND) / TOT, after = ((J + 1) * ND) / TOT;
  if constexpr (after > before) k_dma<NB, WO, before>(d); else k_read<NT, LSTR, RO, J - before>(n, ad);
  if constexpr (J == 0) set_m0<WO>(d.lda);
}
template <int NT, int NB, bool VT, int LSTR, int RO, int WO, int VM, int I, int... J>
DI void k_gap(f32x4 (&acc)[NT][4], const Frag<NT>& c, Frag<NT>& n, const Adr& ad, Dma& d, std::integer_sequence<int, J...>) {
  constexpr int NM = 4 * NT, ND = 2 + NB, TOT = 4 + 2 * NT + ND, SPAN = NM - KTAIL;
  constexpr int t = I >> 2, m = (t & 1) ? 3 - (I & 3) : (I & 3);
  if constexpr (VT && (t == 2 || t == 3)) mfma_acc(acc[t][m], c.a[m], cat(c.b[t][0], c.b[t][1]));
  else mfma_acc(acc[t][m], cat(c.b[t][0], c.b[t][1]), c.a[m]);
  SB();
  if constexpr (I == NM - 1) __builtin_amdgcn_s_setprio(3);
  if constexpr (I == SPAN / 3) __builtin_amdgcn_s_setprio(2);
  if constexpr (I == 2 * SPAN / 3) __builtin_amdgcn_s_setprio(1);
  if constexpr (I == SPAN) __builtin_amdgcn_s_setprio(0);
  (((J * SPAN / TOT == I) ? k_memop<NT, NB, LSTR, RO, WO, J>(n, ad, d) : (void)0), ...);
  if constexpr (I == NM - 2) { wait_lgkm0(); wait_vm<VM * ND>(); }
  SB();
}
template <int NT, int NB, bool VT, int LSTR, int RO, int WO, int VM, int... I>
DI void k_gaps(f32x4 (&acc)[NT][4], const Frag<NT>& c, Frag<NT>& n, const Adr& ad, Dma& d, std::integer_sequence<int, I...>) {
  (k_gap<NT, NB, VT, LSTR, RO, WO, VM, I>(acc, c, n, ad, d, std::make_integer_sequence<int, 4 + 2 * NT + 2 + NB>{}), ...);
}
template <int NT, int NB, bool VT, int LSTR, int RO, int WO, int VM> DI void k_step(f32x4 (&acc)[NT][4], const Frag<NT>& c, Frag<NT>& n, const Adr& ad, Dma& d) {
  bar(); SB();
  k_gaps<NT, NB, VT, LSTR, RO, WO, VM>(acc, c, n, ad, d, std::make_integer_sequence<int, 4 * NT>{});
}
DI Adr adr_at(const Adr& b, unsigned o) { Adr r = {b.a + o, b.l0 + o, b.l1 + o, b.lx + o}; asm volatile("" : "+v"(r.a), "+v"(r.l0), "+v"(r.l1), "+v"(r.lx)); return r; }
constexpr unsigned BIAS = 4096;
DI i32x4 make_srd_b(const void* p, unsigned bytes) { return make_srd((const char*)p - BIAS, bytes + BIAS); }

constexpr int G1_SLOT = 31744, G1_EXTRA = G1_SLOT * 4, G1_SCR = G1_EXTRA + 5120, G1_LDS = G1_SCR + 256;
struct Pf { i32x4 sq, s1, s2, so; unsigned vq, v1; int o2, i; };
template <int NT, int NB, bool KV> DI void g1_mainloop(f32x4 (&acc)[NT][4], const Adr& ad0, Dma& d, const Pf& pf, int lane ST_ARG) {
  constexpr int ND = 2 + NB;
  k_dma_all<NB, 0>(d); k_dma_all<NB, G1_SLOT>(d); k_dma_all<NB, 2 * G1_SLOT>(d); k_dma_all<NB, 3 * G1_SLOT>(d);
  const Adr ad1 = adr_at(ad0, 2 * G1_SLOT);
  Frag<NT> f0, f1;
  wait_vm<3 * ND>(); bar();
  k_read_all<NT, 4096, 0>(f0, ad0, std::make_integer_sequence<int, 4 + 2 * NT>{});
  wait_lgkm0(); wait_vm<2 * ND>();
  ST_MARK(1);
  for (int it = 0; it < 8; ++it) {
    k_step<NT, NB, KV, 4096, G1_SLOT, 0, 2>(acc, f0, f1, ad0, d);
    k_step<NT, NB, KV, 4096, 0, G1_SLOT, 2>(acc, f1, f0, ad1, d);
    k_step<NT, NB, KV, 4096, G1_SLOT, 2 * G1_SLOT, 2>(acc, f0, f1, ad1, d);
    k_step<NT, NB, KV, 4096, 0, 3 * G1_SLOT, 2>(acc, f1, f0, ad0, d);
    if constexpr (NB == 0) {
      if (pf.i >= 0 && (pf.i >> 2) == it && pf.i + 8 * G1_PF0 < 32) {
#pragma unroll
        for (int j = 0; j < 4; ++j) { touch(pf.sq, pf.vq, ((pf.i + 8 * G1_PF0) * 32 + j * 8) * 6144, G1_SCR); touch(pf.s1, pf.v1, ((pf.i + 8 * G1_PF0) * 32 + j * 8) * 7168, G1_SCR); }
      }
      if (pf.i >= 0 && (pf.i < 24 ? pf.i >> 2 : ((pf.i - 24) * 3) >> 2) == it) {
        const int n = pf.i < 24 ? 3 : 9, p0 = pf.o2 + (pf.i < 24 ? pf.i * 3 : 72 + (pf.i - 24) * 9);
        for (int j = 0; j < n; ++j) { const int pc = p0 + j; if (pc < 896) touch(pf.s2, lane * 128, pc * 8192, G1_SCR); else touch(pf.so, lane * 128, (pc - 896) * 8192, G1_SCR); }
      }
    }
  }
  __builtin_amdgcn_s_setprio(0);
  acc_fence();
  wait_vm<0>();
}

DI float max3f(float a, float b, float c) { return __builtin_fmaxf(__builtin_fmaxf(a, b), c); }
DI float max2f(float a, float b) { return __builtin_fmaxf(a, b); }
DI void swap16(float& a, float& b) { auto r = __builtin_amdgcn_permlane16_swap(__float_as_uint(a), __float_as_uint(b), false, false); a = __uint_as_float(r[0]); b = __uint_as_float(r[1]); }
DI void swap32(float& a, float& b) { auto r = __builtin_amdgcn_permlane32_swap(__float_as_uint(a), __float_as_uint(b), false, false); a = __uint_as_float(r[0]); b = __uint_as_float(r[1]); }
DI void dual_exp(const f32x4 (&p)[4][2], bf16x8 (&pos)[4], bf16x8 (&neg)[4]) {
  constexpr float L2E = 1.4426950408889634f;
  float mx[4], nm[4];
#pragma unroll
  for (int m = 0; m < 4; ++m) {
    mx[m] = max2f(max3f(max3f(p[m][0][0], p[m][0][1], p[m][0][2]), p[m][0][3], p[m][1][0]), max3f(p[m][1][1], p[m][1][2], p[m][1][3]));
    nm[m] = max2f(max3f(max3f(-p[m][0][0], -p[m][0][1], -p[m][0][2]), -p[m][0][3], -p[m][1][0]), max3f(-p[m][1][1], -p[m][1][2], -p[m][1][3]));
  }
  float a[4];
#pragma unroll
  for (int m = 0; m < 4; ++m) { swap16(mx[m], nm[m]); a[m] = max2f(mx[m], nm[m]); }
  swap32(a[0], a[1]); swap32(a[2], a[3]);
  float b0 = max2f(a[0], a[1]) * -L2E, b1 = max2f(a[2], a[3]) * -L2E;
  a[0] = b0; a[1] = b0; a[2] = b1; a[3] = b1;
  swap32(a[0], a[1]); swap32(a[2], a[3]);
#pragma unroll
  for (int m = 0; m < 4; ++m) { mx[m] = a[m]; nm[m] = a[m]; swap16(mx[m], nm[m]); }
#pragma unroll
  for (int m = 0; m < 4; ++m) {
    f32x4 e0, e1, n0, n1;
#pragma unroll
    for (int r = 0; r < 4; ++r) {
      e0[r] = __builtin_amdgcn_exp2f(__builtin_fmaf(p[m][0][r], L2E, mx[m])); e1[r] = __builtin_amdgcn_exp2f(__builtin_fmaf(p[m][1][r], L2E, mx[m]));
      n0[r] = __builtin_amdgcn_exp2f(__builtin_fmaf(p[m][0][r], -L2E, nm[m])); n1[r] = __builtin_amdgcn_exp2f(__builtin_fmaf(p[m][1][r], -L2E, nm[m]));
    }
    pos[m] = pack8(e0, e1); neg[m] = pack8(n0, n1);
  }
}
DI u32x4 frag_natural(unsigned x0, unsigned x1, unsigned y0, unsigned y1) {
  auto r0 = __builtin_amdgcn_permlane16_swap(x0, y0, false, false);
  auto r1 = __builtin_amdgcn_permlane16_swap(x1, y1, false, false);
  u32x4 o = {r0[0], r1[0], r0[1], r1[1]};
  return o;
}
DI u32x2 ffn_act(const f32x4& a, const f32x4& b) {
  const f32x4 v = a + b;
  u32x2 r = {cvtpk(v[0] * max2f(v[0], 0.f), v[1] * max2f(v[1], 0.f)), cvtpk(v[2] * max2f(v[2], 0.f), v[3] * max2f(v[3], 0.f))};
  return r;
}
DI f32x4 lds_bias(unsigned addr) { const u32x2 v = *(const LAS u32x2*)(addr); f32x4 r = {lo_f(v[0]), hi_f(v[0]), lo_f(v[1]), hi_f(v[1])}; return r; }
DI void spin_ge(GAS unsigned* w, unsigned want) {
  for (unsigned n = 0; n < (1u << 22); ++n) {
    if ((unsigned)__builtin_amdgcn_readfirstlane(__hip_atomic_load(w, __ATOMIC_RELAXED, __HIP_MEMORY_SCOPE_AGENT)) >= want) return;
    __builtin_amdgcn_s_sleep(1);
  }
}
#define H_AUX 16
template <class R> DI void ffn_pair2(const f32x4 (&t0)[4], const f32x4 (&t1)[4], unsigned bias_addr, const R& rs, unsigned off) {
  const f32x4 b0 = lds_bias(bias_addr), b1 = lds_bias(bias_addr + 32);
#pragma unroll
  for (int m = 0; m < 4; ++m) { const u32x2 x = ffn_act(t0[m], b0), y = ffn_act(t1[m], b1); __builtin_amdgcn_raw_buffer_store_b128(frag_natural(x[0], x[1], y[0], y[1]), rs, off + m * 1024, 0, H_AUX); }
}
template <bool KV> DI void g1_body(char* __restrict__ ws, const u16* __restrict__ wqkv, const u16* __restrict__ wphi, const u16* __restrict__ w1,
                                   const u16* __restrict__ b1, const u16* __restrict__ w2, const u16* __restrict__ wo, int rb, int xc, int tl ST_ARG) {
  constexpr int NT = KV ? 6 : 7;
  const int tid = threadIdx.x, wid = __builtin_amdgcn_readfirstlane(tid >> 6), lane = tid & 63, wm = wid & 3, grp = wid >> 2;
  const int g = lane >> 4, q = (lane & 15) >> 2, p = lane & 3;
  const int hp = head_pair(xc, tl);
  const int fl1 = (tl & 1) * 192, fl2 = KV ? 320 + (tl & 1) * 64 : fl1 + 64, fx = 128 + (tl & 1) * 32;
  f32x4 acc[NT][4];
#pragma unroll
  for (int t = 0; t < NT; ++t)
#pragma unroll
    for (int m = 0; m < 4; ++m) acc[t][m] = (f32x4){0.f, 0.f, 0.f, 0.f};
  Adr ad; Dma d; Pf pf;
  ad.a = wm * 4096 + lane * 16;
  const unsigned lp = (g & 1) * 512 + q * 64 + (g & 1) * 32 + p * 8;
  ad.l0 = 16384 + grp * 2048 + (g >> 1) * 1024 + lp; ad.l1 = ad.l0 ^ 32; ad.lx = (16384 + 12288 + (g >> 1) * 2048 + lp) ^ (grp * 32);
  d.sa = make_srd(ws + XN_OFF, S_ * D_ * 2); d.va0 = d.va1 = lane * 16; d.soa = (rb * 16 + 2 * wid) * 1024; d.stepa = 131072; d.lda = wid * 2048;
  {
    const int line = wid >> 1;
    const bool isq = line == 0 || (KV && line == 1);
    const unsigned pitch = isq ? 6144u : 7168u;
    d.sb = isq ? make_srd_b(wqkv, 1024u * 6144u) : make_srd_b(w1, 1024u * 7168u);
    const int r = lane >> 2, c = (lane & 3) ^ (((r >> 3) & 1) << 1);
    d.vb0 = BIAS + r * pitch + c * 16;
    d.vb1 = d.vb0 - 2048 + (line < 3 ? 64u : 16u * pitch);
    const int col = isq ? (KV ? line + 1 : 0) * 1024 + hp * 64 : xc * 448 + (line == 1 ? fl1 : line == 2 ? fl2 : fx);
    d.sob = col * 2 + (wid & 1) * 16 * pitch; d.stepb = 32 * pitch; d.ldb = 16384 + line * 4096 + (wid & 1) * 1024;
  }
  pf.i = -1;
  if (wid == 7) {
    dma16<0>(make_srd(b1, F_ * 2), lane * 16, xc * 896, G1_EXTRA + 4096);
    const i32x4 sp = make_srd(wphi, H_ * 2048);
    dma16<0>(sp, lane * 16, hp * 4096, G1_EXTRA); dma16<1024>(sp, lane * 16, hp * 4096, G1_EXTRA); dma16<2048>(sp, lane * 16, hp * 4096, G1_EXTRA); dma16<3072>(sp, lane * 16, hp * 4096, G1_EXTRA);
    pf.i = rb * 4 + tl;
  }
  pf.sq = make_srd(wqkv, 1024u * 6144u); pf.s1 = make_srd(w1, 1024u * 7168u);
  pf.vq = lane < 48 ? (lane / 6) * 6144u + qkv_col(xc, lane % 6) : OOB; pf.v1 = lane < 56 ? (lane / 7) * 7168u + xc * 896 + (lane % 7) * 128 : OOB;
  pf.s2 = make_srd(w2, F_ * 2048u); pf.so = make_srd(wo, 1024u * 2048u); pf.o2 = xc * 144;
  if (wid < NT) g1_mainloop<NT, 2, KV>(acc, ad, d, pf, lane ST_PASS);
  else g1_mainloop<NT, 0, KV>(acc, ad, d, pf, lane ST_PASS);
  ST_MARK(2);
  bar();
  const int hd = hp * 2 + grp;
  const f32x4 zero = {0.f, 0.f, 0.f, 0.f};
  const int G = ((g & 1) << 1) | (g >> 1);
  const auto ra2 = __builtin_amdgcn_make_buffer_rsrc(ws + A2_OFF, 0, (unsigned)A2_BYTES, 0x00020000);
  const unsigned a2r = (rb * KS2 * 16 + wm * 4) * 1024 + (lane & 15) * 16, a2b = a2r + xc * 14 * 16384;
  const unsigned bb = G1_EXTRA + 4096 + g * 8;
  if constexpr (!KV) {
    ffn_pair2(acc[2], acc[3], bb + (fl1 + grp * 32) * 2, ra2, a2b + ((fl1 >> 5) + grp) * 16384 + G * 256);
    ffn_pair2(acc[4], acc[5], bb + (fl2 + grp * 32) * 2, ra2, a2b + ((fl2 >> 5) + grp) * 16384 + G * 256);
    const f32x4 b0 = lds_bias(bb + (fx + grp * 16) * 2);
#pragma unroll
    for (int m = 0; m < 4; ++m) __builtin_amdgcn_raw_buffer_store_b64(ffn_act(acc[NT - 1][m], b0), ra2, a2b + (fx >> 5) * 16384 + (grp * 2 + (g >> 1)) * 256 + (g & 1) * 8 + m * 1024, 0, H_AUX);
    SB();
  }
  bf16x8 pos[4], neg[4];
  {
    bf16x8 wp[2];
#pragma unroll
    for (int ft = 0; ft < 2; ++ft) { const unsigned a = G1_EXTRA + grp * 2048 + (4 * g + q) * 64 + ft * 32 + p * 8; wp[ft] = cat(lds_tr(a), lds_tr(a + 1024)); }
    f32x4 pp[4][2];
#pragma unroll
    for (int m = 0; m < 4; ++m) { const bf16x8 qf = pack8(acc[0][m], acc[1][m]); pp[m][0] = MFMA(wp[0], qf, zero); pp[m][1] = MFMA(wp[1], qf, zero); }
    dual_exp(pp, pos, neg);
  }
  const u32x4 onesu = {0x3f803f80u, 0x3f803f80u, 0x3f803f80u, 0x3f803f80u};
  const bf16x8 ones = __builtin_bit_cast(bf16x8, onesu);
  GAS unsigned* cnt = (GAS unsigned*)(ws + FLG_OFF) + hp * 64;
  const auto rkv = __builtin_amdgcn_make_buffer_rsrc(ws + KVP_OFF, 0, H_ * 8 * KVP_STRIDE * 4, 0x00020000);
  const int t2 = tid & 255;
  if constexpr (KV) {
    bf16x8 idm[2];
#pragma unroll
    for (int ft = 0; ft < 2; ++ft) {
      const unsigned one = (g == q) ? ((p & 1) ? 0x3f800000u : 0x00003f80u) : 0u;
      u32x4 iv = {0u, 0u, 0u, 0u};
      iv[ft * 2] = (p >> 1) == 0 ? one : 0u; iv[ft * 2 + 1] = (p >> 1) == 1 ? one : 0u;
      idm[ft] = __builtin_bit_cast(bf16x8, iv);
    }
    f32x4 kv[4][2], zz[4];
#pragma unroll
    for (int f4 = 0; f4 < 4; ++f4) { kv[f4][0] = zero; kv[f4][1] = zero; zz[f4] = zero; }
#pragma unroll
    for (int u = 0; u < 2; ++u) {
      f32x4 tr[2][4];
#pragma unroll
      for (int mm = 0; mm < 2; ++mm) {
        const int m = 2 * u + mm;
        const f32x4 sp = MFMA(pos[m], ones, zero), sn = MFMA(neg[m], ones, zero);
        f32x4 rp, rn;
#pragma unroll
        for (int r = 0; r < 4; ++r) { rp[r] = __builtin_amdgcn_rcpf(sp[r]); rn[r] = __builtin_amdgcn_rcpf(sn[r]); }
        tr[mm][0] = MFMA(pos[m], idm[0], zero) * rp; tr[mm][1] = MFMA(pos[m], idm[1], zero) * rp;
        tr[mm][2] = MFMA(neg[m], idm[0], zero) * rn; tr[mm][3] = MFMA(neg[m], idm[1], zero) * rn;
      }
      const bf16x8 v0 = pack8(acc[2][2 * u], acc[2][2 * u + 1]), v1 = pack8(acc[3][2 * u], acc[3][2 * u + 1]);
#pragma unroll
      for (int f4 = 0; f4 < 4; ++f4) {
        const bf16x8 ph = pack8(tr[0][f4], tr[1][f4]);
        kv[f4][0] = MFMA(ph, v0, kv[f4][0]); kv[f4][1] = MFMA(ph, v1, kv[f4][1]); zz[f4] = MFMA(ph, ones, zz[f4]);
      }
    }
    const unsigned red = wid * (KVP_STRIDE * 4);
#pragma unroll
    for (int f4 = 0; f4 < 4; ++f4) {
#pragma unroll
      for (int t = 0; t < 2; ++t) *(LAS f32x4*)(red + (((f4 >> 1) * 2 + t) * 64 + lane) * 32 + (f4 & 1) * 16) = kv[f4][t];
      if ((lane & 15) == 0) *(LAS f32x4*)(red + 8192 + ((f4 >> 1) * 4 + g) * 32 + (f4 & 1) * 16) = zz[f4];
    }
    wait_lgkm0();
    bar();
    const unsigned src = grp * 4 * (KVP_STRIDE * 4);
#pragma unroll
    for (int i = 0; i < 3; ++i) {
      const int e = (t2 + 256 * i) * 4;
      if (i < 2 || e < KVP_STRIDE) {
        f32x4 sv = *(const LAS f32x4*)(src + e * 4);
#pragma unroll
        for (int w = 1; w < 4; ++w) sv += *(const LAS f32x4*)(src + w * (KVP_STRIDE * 4) + e * 4);
        __builtin_amdgcn_raw_buffer_store_b128(__builtin_bit_cast(u32x4, sv), rkv, ((hd * 8 + rb) * KVP_STRIDE + e) * 4, 0, 16);
      }
    }
    ST_MARK(3);
    wait_vm<0>();
    bar();
    if (tid == 0) __hip_atomic_fetch_add(cnt, 1u, __ATOMIC_RELAXED, __HIP_MEMORY_SCOPE_AGENT);
    ST_MARK(4);
    ffn_pair2(acc[4], acc[5], bb + (fl2 + grp * 32) * 2, ra2, a2b + ((fl2 >> 5) + grp) * 16384 + G * 256);
  } else {
    float rp[4], rn[4];
#pragma unroll
    for (int m = 0; m < 4; ++m) { rp[m] = __builtin_amdgcn_rcpf(MFMA(ones, pos[m], zero)[0]); rn[m] = __builtin_amdgcn_rcpf(MFMA(ones, neg[m], zero)[0]); }
    asm volatile("" : "+v"(rp[0]), "+v"(rp[1]), "+v"(rp[2]), "+v"(rp[3]), "+v"(rn[0]), "+v"(rn[1]), "+v"(rn[2]), "+v"(rn[3]));
    ST_MARK(3);
    if (wid == 0) spin_ge(cnt, 8u);
    bar();
    __builtin_amdgcn_fence(__ATOMIC_ACQUIRE, "wavefront");
    ST_MARK(4);
    f32x4 s0, s1;
    {
      f32x4 pv[8][2];
#pragma unroll
      for (int b = 0; b < 8; ++b) {
        pv[b][0] = __builtin_bit_cast(f32x4, __builtin_amdgcn_raw_buffer_load_b128(rkv, ((hd * 8 + b) * KVP_STRIDE + t2 * 4) * 4, 0, 16));
        pv[b][1] = __builtin_bit_cast(f32x4, __builtin_amdgcn_raw_buffer_load_b128(rkv, ((hd * 8 + b) * KVP_STRIDE + 1024 + t2 * 4) * 4, 0, 16));
      }
      if (t2 < 16) {
        f32x4 pz[8];
#pragma unroll
        for (int b = 0; b < 8; ++b) pz[b] = __builtin_bit_cast(f32x4, __builtin_amdgcn_raw_buffer_load_b128(rkv, ((hd * 8 + b) * KVP_STRIDE + 2048 + t2 * 4) * 4, 0, 16));
        const f32x4 z = ((pz[0] + pz[1]) + (pz[2] + pz[3])) + ((pz[4] + pz[5]) + (pz[6] + pz[7]));
        *(LAS u32x2*)(grp * 8192 + 4096 + t2 * 8) = (u32x2){cvtpk(z[0], z[1]), cvtpk(z[2], z[3])};
      }
      s0 = ((pv[0][0] + pv[1][0]) + (pv[2][0] + pv[3][0])) + ((pv[4][0] + pv[5][0]) + (pv[6][0] + pv[7][0]));
      s1 = ((pv[0][1] + pv[1][1]) + (pv[2][1] + pv[3][1])) + ((pv[4][1] + pv[5][1]) + (pv[6][1] + pv[7][1]));
    }
    *(LAS u32x2*)(grp * 8192 + t2 * 8) = (u32x2){cvtpk(s0[0], s0[1]), cvtpk(s0[2], s0[3])};
    *(LAS u32x2*)(grp * 8192 + 2048 + t2 * 8) = (u32x2){cvtpk(s1[0], s1[1]), cvtpk(s1[2], s1[3])};
    wait_lgkm0();
    bar();
    bf16x8 kv[2][2], zf[2];
#pragma unroll
    for (int pn = 0; pn < 2; ++pn) {
      kv[pn][0] = lds_b128(grp * 8192 + (pn * 2) * 1024 + lane * 16); kv[pn][1] = lds_b128(grp * 8192 + (pn * 2 + 1) * 1024 + lane * 16);
      zf[pn] = lds_b128(grp * 8192 + 4096 + (pn * 4 + g) * 16);
    }
#pragma unroll
    for (int m = 0; m < 4; ++m) {
      const f32x4 np0 = MFMA(kv[0][0], pos[m], zero), np1 = MFMA(kv[0][1], pos[m], zero), nn0 = MFMA(kv[1][0], neg[m], zero), nn1 = MFMA(kv[1][1], neg[m], zero);
      const f32x4 dp = MFMA(zf[0], pos[m], zero), dn = MFMA(zf[1], neg[m], zero);
      const float inv = __builtin_amdgcn_rcpf(dp[0] * rp[m] + dn[0] * rn[m] + 1e-6f);
      const float cp = rp[m] * inv, cn = rn[m] * inv;
      const f32x4 o0 = np0 * cp + nn0 * cn, o1 = np1 * cp + nn1 * cn;
      __builtin_amdgcn_raw_buffer_store_b128(frag_natural(cvtpk(o0[0], o0[1]), cvtpk(o0[2], o0[3]), cvtpk(o1[0], o1[1]), cvtpk(o1[2], o1[3])), ra2, a2r + (112 + hd) * 16384 + G * 256 + m * 1024, 0, H_AUX);
    }
  }
  ST_MARK(5);
  ST_FIN(6);
}
__global__ __launch_bounds__(512, 2) void k_gemm1(char* __restrict__ ws, const u16* __restrict__ wqkv, const u16* __restrict__ wphi, const u16* __restrict__ w1,
                                                  const u16* __restrict__ b1, const u16* __restrict__ w2, const u16* __restrict__ wo) {
  ST_INIT(ws, 1);
  const int xc = blockIdx.x & 7, idx = blockIdx.x >> 3, rb = idx >> 2, tl = idx & 3;
  if (tl < 2) g1_body<true>(ws, wqkv, wphi, w1, b1, w2, wo, rb, xc, tl ST_PASS);
  else g1_body<false>(ws, wqkv, wphi, w1, b1, w2, wo, rb, xc, tl ST_PASS);
}

constexpr int G2_SLOT = 24576, G2_LDS = G2_SLOT * 6;
__global__ __launch_bounds__(512, 2) void k_gemm2(char* __restrict__ ws, const u16* __restrict__ w2, const u16* __restrict__ wo, const u16* __restrict__ x,
                                                  const u16* __restrict__ b2, u16* __restrict__ out) {
  ST_INIT(ws, 3);
  const int tid = threadIdx.x, wid = __builtin_amdgcn_readfirstlane(tid >> 6), lane = tid & 63;
  const int wm = wid & 3, wn = wid >> 2;
  const int xc = blockIdx.x & 7, idx = blockIdx.x >> 3;
  const int kq = xc >> 1, rb = (xc & 1) * 4 + (idx & 3), ct = idx >> 2;
  const int g = lane >> 4, q = (lane & 15) >> 2, p = lane & 3;
  Adr ad0; Dma d;
  ad0.a = wm * 4096 + lane * 16;
  ad0.l0 = 16384 + wn * 4096 + (g >> 1) * 1024 + (g & 1) * 512 + q * 64 + (g & 1) * 32 + p * 8; ad0.l1 = ad0.l0 ^ 32; ad0.lx = 0;
  d.sa = make_srd_b(ws + A2_OFF, (rb * KS2 + kq * 36 + 36) * 16384);
  d.va0 = BIAS + lane * 16 + (((wid & 1) * 2 + kq) & 3) * 1024; d.va1 = BIAS + lane * 16 + (((wid & 1) * 2 + 1 + kq) & 3) * 1024 - 1024;
  d.soa = ((rb * KS2 + kq * 36) * 16 + (wid >> 1) * 4) * 1024; d.stepa = 16384; d.lda = wid * 2048;
  d.sb = make_srd(w2, (kq == 3 ? F_ : (kq + 1) * 1152) * 2048u);
  { const int r = lane >> 2, c = (lane & 3) ^ (((r >> 3) & 1) << 1); d.vb0 = d.vb1 = r * 2048 + c * 16; }
  const unsigned sob0 = (wid >> 2) * 16 * 2048 + (ct * 128 + (wid & 3) * 32) * 2;
  d.sob = sob0 + kq * 1152 * 2048; d.stepb = 32 * 2048; d.ldb = 16384 + (wid & 3) * 2048 + (wid >> 2) * 1024;
  k_dma_all<1, 0>(d); k_dma_all<1, G2_SLOT>(d); k_dma_all<1, 2 * G2_SLOT>(d); k_dma_all<1, 3 * G2_SLOT>(d);
  if (kq == 3) { d.sb = make_srd(wo, 1024u * 2048u); d.sob = sob0; }
  k_dma_all<1, 4 * G2_SLOT>(d); k_dma_all<1, 5 * G2_SLOT>(d);
  f32x4 acc[4][4];
#pragma unroll
  for (int t = 0; t < 4; ++t)
#pragma unroll
    for (int m = 0; m < 4; ++m) acc[t][m] = (f32x4){0.f, 0.f, 0.f, 0.f};
  {
    const Adr ad1 = adr_at(ad0, 2 * G2_SLOT), ad2 = adr_at(ad0, 4 * G2_SLOT);
    Frag<4> f0, f1;
    wait_vm<15>(); bar();
    k_read_all<4, 2048, 0>(f0, ad0, std::make_integer_sequence<int, 12>{});
    wait_lgkm0(); wait_vm<12>();
    ST_MARK(1);
    for (int it = 0; it < 6; ++it) {
      k_step<4, 1, false, 2048, G2_SLOT, 0, 4>(acc, f0, f1, ad0, d);
      k_step<4, 1, false, 2048, 0, G2_SLOT, 4>(acc, f1, f0, ad1, d);
      k_step<4, 1, false, 2048, G2_SLOT, 2 * G2_SLOT, 4>(acc, f0, f1, ad1, d);
      k_step<4, 1, false, 2048, 0, 3 * G2_SLOT, 4>(acc, f1, f0, ad2, d);
      k_step<4, 1, false, 2048, G2_SLOT, 4 * G2_SLOT, 4>(acc, f0, f1, ad2, d);
      k_step<4, 1, false, 2048, 0, 5 * G2_SLOT, 4>(acc, f1, f0, ad0, d);
    }
    __builtin_amdgcn_s_setprio(0);
    acc_fence();
  }
  ST_MARK(2);
  GAS unsigned* flags = (GAS unsigned*)(ws + FLG_OFF) + 16 * 64;
  const auto rx = __builtin_amdgcn_make_buffer_rsrc(ws + XCH_OFF, 0, 256u * 49152u, 0x00020000);
#pragma unroll
  for (int i = 1; i < 4; ++i)
#pragma unroll
    for (int h = 0; h < 2; ++h)
      __builtin_amdgcn_raw_buffer_store_b128(__builtin_bit_cast(u32x4, pack8(acc[2 * h][i], acc[2 * h + 1][i])), rx, blockIdx.x * 49152 + (((i - 1) * 8 + wid) * 2 + h) * 1024 + lane * 16, 0, 16);
  const int G = ((g & 1) << 1) | (g >> 1);
  const size_t go = (size_t)(rb * 256 + wm * 64 + kq * 16 + (lane & 15)) * D_ + ct * 128 + wn * 64 + G * 8;
  u32x4 xv[2], bv[2];
#pragma unroll
  for (int h = 0; h < 2; ++h) { xv[h] = *(const u32x4*)(x + go + h * 32); bv[h] = *(const u32x4*)(b2 + ct * 128 + wn * 64 + G * 8 + h * 32); }
  wait_vm<0>();
  bar();
  ST_MARK(3);
  if (wid == 0) {
    if (lane == 0) __hip_atomic_store(flags + blockIdx.x * 64, 1u, __ATOMIC_RELAXED, __HIP_MEMORY_SCOPE_AGENT);
    GAS unsigned* fp = flags + ((blockIdx.x & ~6) | ((lane & 3) << 1)) * 64;
    for (unsigned n = 0; n < (1u << 22); ++n) {
      if (__all(__hip_atomic_load(fp, __ATOMIC_RELAXED, __HIP_MEMORY_SCOPE_AGENT) != 0u)) break;
      __builtin_amdgcn_s_sleep(1);
    }
  }
  bar();
  __builtin_amdgcn_fence(__ATOMIC_ACQUIRE, "wavefront");
  ST_MARK(4);
  u32x4 pr[3][2];
#pragma unroll
  for (int j = 1; j < 4; ++j) {
    const unsigned src = (blockIdx.x & ~6) | (((kq + j) & 3) << 1);
#pragma unroll
    for (int h = 0; h < 2; ++h) pr[j - 1][h] = __builtin_amdgcn_raw_buffer_load_b128(rx, src * 49152 + (((3 - j) * 8 + wid) * 2 + h) * 1024 + lane * 16, 0, 16);
  }
#pragma unroll
  for (int h = 0; h < 2; ++h) {
    f32x4 v0 = acc[2 * h][0], v1 = acc[2 * h + 1][0];
#pragma unroll
    for (int j = 0; j < 3; ++j) {
      v0 += (f32x4){lo_f(pr[j][h][0]), hi_f(pr[j][h][0]), lo_f(pr[j][h][1]), hi_f(pr[j][h][1])};
      v1 += (f32x4){lo_f(pr[j][h][2]), hi_f(pr[j][h][2]), lo_f(pr[j][h][3]), hi_f(pr[j][h][3])};
    }
#pragma unroll
    for (int r = 0; r < 4; ++r) { float a = v0[r], b = v1[r]; swap16(a, b); v0[r] = a; v1[r] = b; }
    u32x4 o;
    o[0] = cvtpk(v0[0] + lo_f(xv[h][0]) + lo_f(bv[h][0]), v0[1] + hi_f(xv[h][0]) + hi_f(bv[h][0]));
    o[1] = cvtpk(v0[2] + lo_f(xv[h][1]) + lo_f(bv[h][1]), v0[3] + hi_f(xv[h][1]) + hi_f(bv[h][1]));
    o[2] = cvtpk(v1[0] + lo_f(xv[h][2]) + lo_f(bv[h][2]), v1[1] + hi_f(xv[h][2]) + hi_f(bv[h][2]));
    o[3] = cvtpk(v1[2] + lo_f(xv[h][3]) + lo_f(bv[h][3]), v1[3] + hi_f(xv[h][3]) + hi_f(bv[h][3]));
    *(u32x4*)(out + go + h * 32) = o;
  }
  ST_MARK(5);
  ST_FIN(6);
}

__global__ void k_report(char* ws, int qsel) {
  const unsigned long long* h = (const unsigned long long*)(ws + STAMP_OFF);
  __shared__ unsigned long long sm[2][256];
  const int b = threadIdx.x;
  unsigned long long g0 = ~0ull, v = 0;
  for (int w = 0; w < 4; ++w) { unsigned long long t = h[((FIRSTK * 256 + b) * 8 + w) * 16 + 15]; g0 = t < g0 ? t : g0; }
  if (qsel >= 0) { const int k = qsel >> 2, f = qsel & 3, nw = (k == 0) ? 4 : 8, ix = f == 0 ? 15 : f == 1 ? 9 : f == 2 ? 13 : 14;
    v = f == 0 ? ~0ull : 0;
    for (int w = 0; w < nw; ++w) { unsigned long long t = h[((k * 256 + b) * 8 + w) * 16 + ix]; v = f == 0 ? (t < v ? t : v) : (t > v ? t : v); } }
  sm[0][b] = g0; sm[1][b] = v; __syncthreads();
  if (b == 0) { for (int i = 1; i < 256; ++i) { g0 = sm[0][i] < g0 ? sm[0][i] : g0; v = (qsel & 3) == 0 ? (sm[1][i] < v ? sm[1][i] : v) : (sm[1][i] > v ? sm[1][i] : v); }
    unsigned long long d = qsel >= 0 ? (v - g0) * 2 : 0; if (d > 100000) d = 100000;
    const unsigned long long t0 = __builtin_amdgcn_s_memrealtime(); while (__builtin_amdgcn_s_memrealtime() - t0 < d) __builtin_amdgcn_s_sleep(1); }
}
extern "C" void kernel_launch(void* const* d_in, const int* in_sizes, int n_in, void* d_out, int out_size, void* d_ws, size_t ws_size, hipStream_t stream) {
  const u16* x = (const u16*)d_in[0]; const u16* wqkv = (const u16*)d_in[1]; const u16* wphi = (const u16*)d_in[2]; const u16* wo = (const u16*)d_in[3];
  const u16* w1 = (const u16*)d_in[5]; const u16* b1 = (const u16*)d_in[6]; const u16* w2 = (const u16*)d_in[7]; const u16* b2 = (const u16*)d_in[8];
  char* ws = (char*)d_ws;
  (void)hipFuncSetAttribute((const void*)k_gemm1, hipFuncAttributeMaxDynamicSharedMemorySize, G1_LDS);
  (void)hipFuncSetAttribute((const void*)k_gemm2, hipFuncAttributeMaxDynamicSharedMemorySize, G2_LDS);
  k_ln<<<256, 256, 0, stream>>>(x, ws, wqkv, w1, wphi, b1);
  k_gemm1<<<256, 512, G1_LDS, stream>>>(ws, wqkv, wphi, w1, b1, w2, wo);
  k_gemm2<<<256, 512, G2_LDS, stream>>>(ws, w2, wo, x, b2, (u16*)d_out);
  k_report<<<1, 256, 0, stream>>>(ws, QSEL);
}
```

```cpp
#include <hip/hip_runtime.h>
#include <utility>

typedef unsigned short u16;
typedef __bf16 bf16x8 __attribute__((ext_vector_type(8)));
typedef __bf16 bf16x2 __attribute__((ext_vector_type(2)));
typedef short s16x4 __attribute__((ext_vector_type(4)));
typedef float f32x2 __attribute__((ext_vector_type(2)));
typedef float f32x4 __attribute__((ext_vector_type(4)));
typedef unsigned u32x2 __attribute__((ext_vector_type(2)));
typedef unsigned u32x4 __attribute__((ext_vector_type(4)));
typedef int i32x4 __attribute__((ext_vector_type(4)));
#define LAS __attribute__((address_space(3)))
#define GAS __attribute__((address_space(1)))
#define DI __device__ __forceinline__
#define SB() __builtin_amdgcn_sched_barrier(0)
#define MFMA(a, b, c) __builtin_amdgcn_mfma_f32_16x16x32_bf16((a), (b), (c), 0, 0, 0)

constexpr int S_ = 2048, D_ = 1024, H_ = 32, F_ = 3584;
constexpr int KS2 = 144;
constexpr size_t XN_OFF = 0;
constexpr size_t A2_OFF = 4u << 20;
constexpr size_t A2_BYTES = (size_t)S_ * 4608 * 2;
constexpr size_t XR_OFF = 24u << 20;
constexpr size_t KVP_OFF = 32u << 20;
constexpr int KVP_STRIDE = 4352;
constexpr size_t XCH_OFF = 36u << 20;
constexpr size_t FLG_OFF = 52u << 20;
constexpr int FL_XN = 1, FL_H = 65, FL_KV = 129, FL_KT = 145, FL_O = 161, FL_X = 193;
constexpr size_t KVT_OFF = 34u << 20;

DI unsigned cvtpk(float lo, float hi) { f32x2 v = {lo, hi}; bf16x2 b = __builtin_convertvector(v, bf16x2); return __builtin_bit_cast(unsigned, b); }
DI float rbf(float x) { return __uint_as_float(cvtpk(x, 0.f) << 16); }
DI float lo_f(unsigned p) { return __uint_as_float(p << 16); }
DI float hi_f(unsigned p) { return __uint_as_float(p & 0xffff0000u); }
DI bf16x8 pack8(const f32x4& a, const f32x4& b) { u32x4 r = {cvtpk(a[0], a[1]), cvtpk(a[2], a[3]), cvtpk(b[0], b[1]), cvtpk(b[2], b[3])}; return __builtin_bit_cast(bf16x8, r); }
DI i32x4 make_srd(const void* p, unsigned bytes) { unsigned long long a = (unsigned long long)p; i32x4 r; r[0] = (int)(unsigned)a; r[1] = (int)((unsigned)(a >> 32) & 0xffffu); r[2] = (int)bytes; r[3] = 0x00020000; return r; }

template <int OFF> DI void dma16(const i32x4& srd, unsigned voff, unsigned soff, unsigned lds) {
  asm volatile("s_mov_b32 m0, %3\n\ts_nop 0\n\tbuffer_load_dwordx4 %0, %1, %2 offen offset:%4 lds" ::"v"(voff), "s"(srd), "s"(soff), "s"(lds), "i"(OFF) : "memory");
}
template <int IMM> DI void set_m0(unsigned base) { asm volatile("s_add_u32 m0, %0, %1" ::"s"(base), "i"(IMM) : "scc"); }
template <int OFF> DI void dma_ld(const i32x4& srd, unsigned voff, unsigned soff) {
  asm volatile("buffer_load_dwordx4 %0, %1, %2 offen offset:%3 lds" ::"v"(voff), "s"(srd), "s"(soff), "i"(OFF) : "memory");
}
template <int OFF> DI void dma_ld_dev(const i32x4& srd, unsigned voff, unsigned soff) {
  asm volatile("buffer_load_dwordx4 %0, %1, %2 offen offset:%3 sc1 lds" ::"v"(voff), "s"(srd), "s"(soff), "i"(OFF) : "memory");
}
DI void nop1() { asm volatile("s_nop 0"); }
DI void touch(const i32x4& srd, unsigned voff, unsigned soff, unsigned lds) {
  asm volatile("s_mov_b32 m0, %3\n\ts_nop 0\n\tbuffer_load_dword %0, %1, %2 offen lds" ::"v"(voff), "s"(srd), "s"(soff), "s"(lds) : "memory");
}
DI void touch_nt(const i32x4& srd, unsigned voff, unsigned soff, unsigned lds) {
  asm volatile("s_mov_b32 m0, %3\n\ts_nop 0\n\tbuffer_load_dword %0, %1, %2 offen nt lds" ::"v"(voff), "s"(srd), "s"(soff), "s"(lds) : "memory");
}
constexpr unsigned OOB = 0x7fffffffu;
template <int N> DI void wait_vm() { asm volatile("s_waitcnt vmcnt(%0)" ::"i"(N) : "memory"); }
DI void wait_lgkm0() { __builtin_amdgcn_s_waitcnt(0xC07F); }
DI void bar() { asm volatile("" ::: "memory"); __builtin_amdgcn_s_barrier(); asm volatile("" ::: "memory"); }
DI bf16x8 lds_b128(unsigned addr) { return *(const LAS bf16x8*)(addr); }
DI s16x4 lds_tr(unsigned addr) { return __builtin_amdgcn_ds_read_tr16_b64_v4i16((LAS s16x4*)(addr)); }
DI bf16x8 cat(const s16x4& a, const s16x4& b) { return __builtin_bit_cast(bf16x8, __builtin_shufflevector(a, b, 0, 1, 2, 3, 4, 5, 6, 7)); }

#define LCN(i)
#define LT_ARG
#define LT_PASS
#define LT(i)
#define LC(i)

DI int head_pair(int xc, int tl) { return ((7 * xc + (tl < 2 ? 7 : 3)) & 7) + 8 * (tl & 1); }
DI unsigned qkv_col(int xc, int c) { return (c >> 1) * 2048 + head_pair(xc, (c < 2 ? 2 : 0) + (c & 1)) * 128; }
DI unsigned qkv_lines(int xc, int i, int lane) { const int r = lane / 6, c = lane % 6; return lane < 48 ? (unsigned)((r * 32 + i) * 6144) + qkv_col(xc, c) : OOB; }
DI unsigned w1_lines(int xc, int i, int lane) { const int r = lane / 7, c = lane % 7; return lane < 56 ? (unsigned)((r * 32 + i) * 7168 + xc * 896 + c * 128) : OOB; }
typedef unsigned long long u64;
typedef GAS u64 gu64;
DI void post(gu64* w, u64 tag) { __hip_atomic_store(w, tag, __ATOMIC_RELAXED, __HIP_MEMORY_SCOPE_AGENT); }
DI void await_all(gu64* w, u64 tag) {
#pragma nounroll
  for (unsigned n = 0; n < (1u << 20); ++n) {
    if (__all(__hip_atomic_load(w, __ATOMIC_RELAXED, __HIP_MEMORY_SCOPE_AGENT) == tag)) return;
    __builtin_amdgcn_s_sleep(1);
  }
}
DI void mfma_acc(f32x4& acc, const bf16x8& a, const bf16x8& b) { asm volatile("v_mfma_f32_16x16x32_bf16 %0, %1, %2, %0" : "+v"(acc) : "v"(a), "v"(b)); }
DI void acc_fence() { SB(); asm volatile("s_nop 7\n\ts_nop 7"); SB(); }
template <int NT> struct Frag { bf16x8 a[4]; s16x4 b[NT][2]; };
struct Adr { unsigned a, l0, l1, lx; };
struct Dma { i32x4 sa, sb; unsigned va0, va1, vb0, vb1, soa, sob, stepa, stepb, lda, ldb; };
#define KTAIL 3
template <int NT, int LSTR, int RO, int R> DI void k_read(Frag<NT>& f, const Adr& ad) {
  if constexpr (R < 2) f.b[0][R] = lds_tr(ad.l0 + RO + R * 256);
  else if constexpr (R < 6) f.a[R - 2] = lds_b128(ad.a + RO + (R - 2) * 1024);
  else {
    constexpr int t = (R - 4) >> 1, e = (R - 4) & 1;
    if constexpr (t < 6) f.b[t][e] = lds_tr(((t & 1) ? ad.l1 : ad.l0) + RO + (t >> 1) * LSTR + e * 256);
    else f.b[t][e] = lds_tr(ad.lx + RO + e * 256);
  }
}
template <int NT, int LSTR, int RO, int... R> DI void k_read_all(Frag<NT>& f, const Adr& ad, std::integer_sequence<int, R...>) { (k_read<NT, LSTR, RO, R>(f, ad), ...); }
template <int NB, int WO, int P> DI void k_dma(Dma& d) {
  if constexpr (P == 0) dma_ld_dev<0>(d.sa, d.va0, d.soa);
  if constexpr (P == 1) { dma_ld_dev<1024>(d.sa, d.va1, d.soa); d.soa += d.stepa; if constexpr (NB > 0) set_m0<WO>(d.ldb); }
  if constexpr (P == 2) { dma_ld<0>(d.sb, d.vb0, d.sob); if constexpr (NB == 1) d.sob += d.stepb; }
  if constexpr (P == 3) { dma_ld<2048>(d.sb, d.vb1, d.sob); d.sob += d.stepb; }
}
template <int WO> DI void k_dma_a(Dma& d) { set_m0<WO>(d.lda); nop1(); k_dma<0, WO, 0>(d); k_dma<0, WO, 1>(d); }
DI void touch(const i32x4& srd, unsigned voff, unsigned soff, unsigned lds);
template <int NB, int WO> DI void k_dma_b(Dma& d) { set_m0<WO>(d.ldb); nop1(); k_dma<NB, WO, 2>(d); if constexpr (NB > 1) k_dma<NB, WO, 3>(d); }
template <int NT, int NB, int LSTR, int RO, int WO, int J> DI void k_memop(Frag<NT>& n, const Adr& ad, Dma& d) {
  constexpr int ND = 2 + NB, TOT = 4 + 2 * NT + ND, before = (J * ND) / TOT, after = ((J + 1) * ND) / TOT;
  if constexpr (after > before) k_dma<NB, WO, before>(d); else k_read<NT, LSTR, RO, J - before>(n, ad);
  if constexpr (J == 0) set_m0<WO>(d.lda);
}
constexpr int G1_SLOT = 31744, G2_SLOT = 24576;
template <int SLOT> DI void g2_b_piece(const Dma& d) { set_m0<SLOT * G2_SLOT>(d.ldb); nop1(); dma_ld<0>(d.sb, d.vb0, d.sob + SLOT * 32 * 2048); }
template <int NT, int NB, bool VT, int LSTR, int RO, int WO, int VM, int LAST, int I, int... J>
DI void k_gap(f32x4 (&acc)[NT][4], const Frag<NT>& c, Frag<NT>& n, const Adr& ad, Dma& d, std::integer_sequence<int, J...>) {
  constexpr int NM = 4 * NT, ND = 2 + NB, TOT = 4 + 2 * NT + (LAST < 0 ? ND : 0), SPAN = NM - KTAIL;
  constexpr int t = I >> 2, m = (t & 1) ? 3 - (I & 3) : (I & 3);
  if constexpr (VT && (t == 2 || t == 3)) mfma_acc(acc[t][m], c.a[m], cat(c.b[t][0], c.b[t][1]));
  else mfma_acc(acc[t][m], cat(c.b[t][0], c.b[t][1]), c.a[m]);
  SB();
  if constexpr (I == NM - 1) __builtin_amdgcn_s_setprio(3);
  if constexpr (I == SPAN / 3) __builtin_amdgcn_s_setprio(2);
  if constexpr (I == 2 * SPAN / 3) __builtin_amdgcn_s_setprio(1);
  if constexpr (I == SPAN) __builtin_amdgcn_s_setprio(0);
  if constexpr (LAST < 0) {
    (((J * SPAN / TOT == I) ? k_memop<NT, NB, LSTR, RO, WO, J>(n, ad, d) : (void)0), ...);
    if constexpr (I == NM - 2) { wait_lgkm0(); wait_vm<VM * ND>(); }
  } else if constexpr (LAST >= 10) {
    if constexpr (LAST == 10) (((J * SPAN / TOT == I) ? k_read<NT, LSTR, RO, J>(n, ad) : (void)0), ...);
    if constexpr (I == NM - 2) { wait_lgkm0(); if constexpr (VM >= 0) wait_vm<VM>(); }
  } else {
    if constexpr (LAST < 3) (((J * SPAN / TOT == I) ? k_read<NT, LSTR, RO, J>(n, ad) : (void)0), ...);
    if constexpr (I == 4) g2_b_piece<LAST>(d);
    if constexpr (I == 12 && LAST == 0) g2_b_piece<5>(d);
    if constexpr (I == 12 && LAST == 3) g2_b_piece<4>(d);
    if constexpr (I == NM - 2) { wait_lgkm0(); if constexpr (LAST == 0) wait_vm<6>(); if constexpr (LAST == 1) wait_vm<3>(); }
  }
  SB();
}
template <int NT, int NB, bool VT, int LSTR, int RO, int WO, int VM, int LAST, int... I>
DI void k_gaps(f32x4 (&acc)[NT][4], const Frag<NT>& c, Frag<NT>& n, const Adr& ad, Dma& d, std::integer_sequence<int, I...>) {
  (k_gap<NT, NB, VT, LSTR, RO, WO, VM, LAST, I>(acc, c, n, ad, d, std::make_integer_sequence<int, 4 + 2 * NT + (LAST < 0 ? 2 + NB : 0)>{}), ...);
}
template <int NT, int NB, bool VT, int LSTR, int RO, int WO, int VM, int LAST = -1> DI void k_step(f32x4 (&acc)[NT][4], const Frag<NT>& c, Frag<NT>& n, const Adr& ad, Dma& d) {
  bar(); SB();
  k_gaps<NT, NB, VT, LSTR, RO, WO, VM, LAST>(acc, c, n, ad, d, std::make_integer_sequence<int, 4 * NT>{});
}
DI Adr adr_at(const Adr& b, unsigned o) { Adr r = {b.a + o, b.l0 + o, b.l1 + o, b.lx + o}; asm volatile("" : "+v"(r.a), "+v"(r.l0), "+v"(r.l1), "+v"(r.lx)); return r; }
constexpr unsigned BIAS = 4096;
DI i32x4 make_srd_b(const void* p, unsigned bytes) { return make_srd((const char*)p - BIAS, bytes + BIAS); }

constexpr int G1_EXTRA = G2_SLOT * 6, G1_SCR = G1_EXTRA + 5120, KVS = G1_SCR + 768, LDS_BYTES = KVS + 2 * 4352;
constexpr int OFL = G1_EXTRA;
constexpr int G1_PF0 = 12;
#define TOUCH1 touch
struct Pf { const u16 *wqkv, *w1, *w2, *wo; int xc, i, wid; };
DI int fresh_lane() { return __builtin_amdgcn_mbcnt_hi(~0u, __builtin_amdgcn_mbcnt_lo(~0u, 0u)); }
template <int NT, bool KV> DI void g1_mainloop(f32x4 (&acc)[NT][4], Frag<NT>& f0, Frag<NT>& f1, const Adr& ad0, const Adr& ad1, Dma& d, const Pf& pf LT_ARG) {
  constexpr int NB = 2;
  for (int it = 0; it < 7; ++it) {
    k_step<NT, NB, KV, 4096, G1_SLOT, 0, 2>(acc, f0, f1, ad0, d);
    k_step<NT, NB, KV, 4096, 0, G1_SLOT, 2>(acc, f1, f0, ad1, d);
    k_step<NT, NB, KV, 4096, G1_SLOT, 2 * G1_SLOT, 2>(acc, f0, f1, ad1, d);
    k_step<NT, NB, KV, 4096, 0, 3 * G1_SLOT, 2>(acc, f1, f0, ad0, d);
    if ((pf.i < 20 ? pf.i >> 2 : (pf.i - 20) >> 1) == it) {
      int lane = fresh_lane();
      asm volatile("" : "+v"(lane));
      if (pf.i + G1_PF0 < 32) {
        if (pf.wid & 1) TOUCH1(make_srd(pf.wqkv, 1024u * 6144u), lane < 48 ? (lane / 6) * 6144u + qkv_col(pf.xc, lane % 6) : OOB, ((pf.i + G1_PF0) * 32 + (pf.wid >> 1) * 8) * 6144, G1_SCR);
        else TOUCH1(make_srd(pf.w1, 1024u * 7168u), lane < 56 ? (lane / 7) * 7168u + pf.xc * 896 + (lane % 7) * 128 : OOB, ((pf.i + G1_PF0) * 32 + (pf.wid >> 1) * 8) * 7168, G1_SCR);
      }
      const int n = pf.i < 20 ? 3 : 7, pc = pf.xc * 144 + (pf.i < 20 ? pf.i * 3 : 60 + (pf.i - 20) * 7) + pf.wid;
      if (pf.wid < n) { if (pc < 896) touch(make_srd(pf.w2, F_ * 2048u), lane * 128, pc * 8192, G1_SCR); else touch(make_srd(pf.wo, 1024u * 2048u), lane * 128, (pc - 896) * 8192, G1_SCR); }
    }
  }
}

DI float max3f(float a, float b, float c) { return __builtin_fmaxf(__builtin_fmaxf(a, b), c); }
DI float max2f(float a, float b) { return __builtin_fmaxf(a, b); }
DI void swap16(float& a, float& b) { auto r = __builtin_amdgcn_permlane16_swap(__float_as_uint(a), __float_as_uint(b), false, false); a = __uint_as_float(r[0]); b = __uint_as_float(r[1]); }
DI void swap32(float& a, float& b) { auto r = __builtin_amdgcn_permlane32_swap(__float_as_uint(a), __float_as_uint(b), false, false); a = __uint_as_float(r[0]); b = __uint_as_float(r[1]); }
constexpr float L2E = 1.4426950408889634f;
DI void dual_max(const f32x4 (&p)[4][2], float (&mx)[4], float (&nm)[4]) {
#pragma unroll
  for (int m = 0; m < 4; ++m) {
    mx[m] = max2f(max3f(max3f(p[m][0][0], p[m][0][1], p[m][0][2]), p[m][0][3], p[m][1][0]), max3f(p[m][1][1], p[m][1][2], p[m][1][3]));
    nm[m] = max2f(max3f(max3f(-p[m][0][0], -p[m][0][1], -p[m][0][2]), -p[m][0][3], -p[m][1][0]), max3f(-p[m][1][1], -p[m][1][2], -p[m][1][3]));
  }
  float a[4];
#pragma unroll
  for (int m = 0; m < 4; ++m) { swap16(mx[m], nm[m]); a[m] = max2f(mx[m], nm[m]); }
  swap32(a[0], a[1]); swap32(a[2], a[3]);
  float b0 = max2f(a[0], a[1]) * -L2E, b1 = max2f(a[2], a[3]) * -L2E;
  a[0] = b0; a[1] = b0; a[2] = b1; a[3] = b1;
  swap32(a[0], a[1]); swap32(a[2], a[3]);
#pragma unroll
  for (int m = 0; m < 4; ++m) { mx[m] = a[m]; nm[m] = a[m]; swap16(mx[m], nm[m]); }
}
DI bf16x8 half_exp(const f32x4 (&p)[2], float c, float off) {
  f32x4 e0, e1;
#pragma unroll
  for (int r = 0; r < 4; ++r) { e0[r] = __builtin_amdgcn_exp2f(__builtin_fmaf(p[0][r], c, off)); e1[r] = __builtin_amdgcn_exp2f(__builtin_fmaf(p[1][r], c, off)); }
  return pack8(e0, e1);
}
DI u32x4 frag_natural(unsigned x0, unsigned x1, unsigned y0, unsigned y1) {
  auto r0 = __builtin_amdgcn_permlane16_swap(x0, y0, false, false);
  auto r1 = __builtin_amdgcn_permlane16_swap(x1, y1, false, false);
  u32x4 o = {r0[0], r1[0], r0[1], r1[1]};
  return o;
}
DI float relu(float v) { const int b = __float_as_int(v); return __int_as_float(b > 0 ? b : 0); }
DI u32x2 ffn_act(const f32x4& v) {
  u32x2 r = {cvtpk(v[0] * relu(v[0]), v[1] * relu(v[1])), cvtpk(v[2] * relu(v[2]), v[3] * relu(v[3]))};
  return r;
}
DI f32x4 lds_bias(unsigned addr) { const u32x2 v = *(const LAS u32x2*)(addr); f32x4 r = {lo_f(v[0]), hi_f(v[0]), lo_f(v[1]), hi_f(v[1])}; return r; }
#define H_AUX 16
template <class R> DI void ffn_pair2(const f32x4 (&t0)[4], const f32x4 (&t1)[4], const R& rs, unsigned off) {
#pragma unroll
  for (int m = 0; m < 4; ++m) { const u32x2 x = ffn_act(t0[m]), y = ffn_act(t1[m]); __builtin_amdgcn_raw_buffer_store_b128(frag_natural(x[0], x[1], y[0], y[1]), rs, off + m * 1024, 0, H_AUX); }
}
DI float sum_8_16_32(float v) {
  v += __uint_as_float(__builtin_amdgcn_update_dpp(0u, __float_as_uint(v), 0x128, 0xf, 0xf, false));
  float w = v; swap16(v, w); v += w;
  w = v; swap32(v, w); return v + w;
}
DI void g1_dma_b(Dma& d, bool kv, int wid, int lane, int xc, int tl, const u16* wqkv, const u16* w1) {
  const int line = wid >> 1, fl1 = (tl & 1) * 192, fl2 = kv ? 320 + (tl & 1) * 64 : fl1 + 64, fx = 128 + (tl & 1) * 32;
  const bool isq = line == 0 || (kv && line == 1);
  const unsigned pitch = isq ? 6144u : 7168u;
  d.sb = isq ? make_srd_b(wqkv, 1024u * 6144u) : make_srd_b(w1, 1024u * 7168u);
  const int r = lane >> 2, c = (lane & 3) ^ (((r >> 3) & 1) << 1);
  d.vb0 = BIAS + r * pitch + c * 16;
  d.vb1 = d.vb0 - 2048 + (line < 3 ? 64u : 16u * pitch);
  const int col = isq ? (kv ? line + 1 : 0) * 1024 + head_pair(xc, tl) * 64 : xc * 448 + (line == 1 ? fl1 : line == 2 ? fl2 : fx);
  d.sob = col * 2 + (wid & 1) * 16 * pitch; d.stepb = 32 * pitch; d.ldb = 16384 + line * 4096 + (wid & 1) * 1024;
}
#define XPOL " nt"
#define EARLY_STEPS 4
DI void first_touches(const u16* wqkv, const u16* w1, int xc, int i, int lane, int k0, int n) {
  touch(make_srd(wqkv, 1024u * 6144u), lane < 6 * n ? qkv_lines(xc, i, lane) : OOB, k0 * 32 * 6144, G1_SCR);
  touch(make_srd(w1, 1024u * 7168u), lane < 7 * n ? w1_lines(xc, i, lane) : OOB, k0 * 32 * 7168, G1_SCR);
}
struct XKeep { u32x4 v0, v1; unsigned off; };
DI u64 start_phase(char* __restrict__ ws, const u16* __restrict__ x, const u16* __restrict__ wqkv, const u16* __restrict__ wphi, const u16* __restrict__ w1,
                   const u16* __restrict__ b1, const u16* __restrict__ b2, int rb, int xc, int tl, XKeep& xk LT_ARG) {
  const int tid = threadIdx.x, wid = __builtin_amdgcn_readfirstlane(tid >> 6), lane = tid & 63;
  gu64* flg = (gu64*)(ws + FLG_OFF);
  constexpr unsigned RED = G1_SCR + 256;
  const int lrow = blockIdx.x * 8 + (tid & 7);
  {
    const i32x4 sx = make_srd(x, S_ * D_ * 2);
    const unsigned vo = lrow * 2048 + (tid >> 3) * 16;
    set_m0<0>(wid * 2048); nop1();
    asm volatile("buffer_load_dwordx4 %0, %1, 0 offen" XPOL " lds\n\tbuffer_load_dwordx4 %0, %1, 0 offen offset:1024" XPOL " lds" ::"v"(vo), "s"(sx) : "memory");
  }
  u64 tag = *(const __attribute__((address_space(4))) u64*)(ws + FLG_OFF);
  const bool kv = tl < 2;
  if (wid < (kv ? 6 : 7)) {
    Dma d; g1_dma_b(d, kv, wid, lane, xc, tl, wqkv, w1); k_dma_b<2, 0>(d); k_dma_b<2, G1_SLOT>(d);
    wait_vm<4>();
  } else if (wid == 7) {
    dma16<0>(make_srd(b1, F_ * 2), lane * 16, xc * 896, G1_EXTRA + 4096);
    const i32x4 sp = make_srd(wphi, H_ * 2048); const unsigned so = head_pair(xc, tl) * 4096;
    dma16<0>(sp, lane * 16, so, G1_EXTRA); dma16<1024>(sp, lane * 16, so, G1_EXTRA); dma16<2048>(sp, lane * 16, so, G1_EXTRA); dma16<3072>(sp, lane * 16, so, G1_EXTRA);
    first_touches(wqkv, w1, xc, rb * 4 + tl, lane, 2, EARLY_STEPS);
    touch(make_srd(b2, D_ * 2), lane < 2 ? lane * 128u : OOB, rb * 256, G1_SCR);
    wait_vm<8>();
  } else wait_vm<0>();
  LT(1);
  float xf[16];
  const u32x4 v0 = *(const LAS u32x4*)(wid * 2048 + lane * 16), v1 = *(const LAS u32x4*)(wid * 2048 + 1024 + lane * 16);
  const unsigned xo = ((((tid >> 3) >> 2) * 128 + (lrow >> 4)) * 64 + ((tid >> 3) & 3) * 16 + (lrow & 15)) * 16;
  {
#pragma unroll
    for (int j = 0; j < 4; ++j) { xf[2 * j] = lo_f(v0[j]); xf[2 * j + 1] = hi_f(v0[j]); xf[8 + 2 * j] = lo_f(v1[j]); xf[9 + 2 * j] = hi_f(v1[j]); }
  }
  float s = 0.f;
#pragma unroll
  for (int j = 0; j < 16; j += 4) s += (xf[j] + xf[j + 1]) + (xf[j + 2] + xf[j + 3]);
  const float mw = sum_8_16_32(s) * (1.f / 128);
  float q0 = 0.f, q1 = 0.f;
#pragma unroll
  for (int j = 0; j < 16; j += 2) { const float a = xf[j] - mw, b = xf[j + 1] - mw; q0 += a * a; q1 += b * b; }
  const float qs = sum_8_16_32(q0 + q1);
  if (lane < 8) *(LAS f32x2*)(RED + (lane * 8 + wid) * 8) = (f32x2){mw, qs};
  wait_lgkm0(); bar();
  {
    f32x4 r[4];
#pragma unroll
    for (int j = 0; j < 4; ++j) r[j] = *(const LAS f32x4*)(RED + (tid & 7) * 64 + j * 16);
    const f32x4 rs = (r[0] + r[1]) + (r[2] + r[3]);
    const float mean = (rs[0] + rs[2]) * 0.125f;
    float dv = 0.f;
#pragma unroll
    for (int j = 0; j < 4; ++j) { const float a = r[j][0] - mean, b = r[j][2] - mean; dv += a * a + b * b; }
    const float var = ((rs[1] + rs[3]) + 128.f * dv) * (1.f / D_);
    const float mb = rbf(mean), inv = __builtin_amdgcn_rcpf(rbf(__builtin_amdgcn_sqrtf(rbf(rbf(var) + 1e-5f))));
    const auto rxn = __builtin_amdgcn_make_buffer_rsrc(ws + XN_OFF, 0, S_ * D_ * 2, 0x00020000);
#pragma unroll
    for (int i = 0; i < 2; ++i) {
      u32x4 o;
#pragma unroll
      for (int j = 0; j < 4; ++j) { const unsigned dd = cvtpk(xf[i * 8 + 2 * j] - mb, xf[i * 8 + 2 * j + 1] - mb); o[j] = cvtpk(lo_f(dd) * inv, hi_f(dd) * inv); }
      __builtin_amdgcn_raw_buffer_store_b128(o, rxn, xo + i * 16 * 131072, 0, 16);
    }
  }
  LT(2);
  tag = (tag ^ (tag >> 29)) * 0x9E3779B97F4A7C15ull + 0x632BE59BD9B4E019ull;
  asm volatile("" : "+s"(tag));
  wait_vm<0>(); bar();
  LT(3);
  if (tid < 8) post(flg + (FL_XN + tid * 8 + (blockIdx.x >> 5)) * 32 + (blockIdx.x & 31), tag);
  xk.v0 = v0; xk.v1 = v1; xk.off = xo;
  return tag;
}
struct QState { bf16x8 pos[4], neg[4]; float rp[4], rn[4]; };
struct G2Pos { int kq, rb, ct; };
DI G2Pos g2_pos() { const int xc = blockIdx.x & 7, idx = blockIdx.x >> 3; G2Pos p = {xc >> 1, (xc & 1) * 4 + (idx & 3), idx >> 2}; return p; }
DI unsigned g2_sob0(int wid, int ct) { return (wid >> 2) * 16 * 2048 + (ct * 128 + (wid & 3) * 32) * 2; }
DI void g2_setup_b(Dma& d, const u16* w2, int wid, int lane) {
  const G2Pos ps = g2_pos();
  d.sb = make_srd(w2, F_ * 2048u);
  { const int r = lane >> 2, c = (lane & 3) ^ (((r >> 3) & 1) << 1); d.vb0 = d.vb1 = r * 2048 + c * 16; }
  d.sob = g2_sob0(wid, ps.ct) + ps.kq * 896 * 2048; d.stepb = 32 * 2048; d.ldb = 16384 + (wid & 3) * 2048 + (wid >> 2) * 1024;
}
template <int SL> DI void g2_first_a(Dma& d) { k_dma_a<SL * G2_SLOT>(d); if constexpr (SL >= 2) touch(d.sa, OOB, 0, G1_SCR); }
DI void g2_setup_a(Dma& d, char* ws, int wid, int lane) {
  const G2Pos ps = g2_pos();
  d.sa = make_srd_b(ws + A2_OFF, (ps.rb * KS2 + 112 + ps.kq * 8 + 8) * 16384);
  d.va0 = BIAS + lane * 16 + (((wid & 1) * 2 + ps.kq) & 3) * 1024; d.va1 = BIAS + lane * 16 + (((wid & 1) * 2 + 1 + ps.kq) & 3) * 1024 - 1024;
  d.soa = ((ps.rb * KS2 + ps.kq * 28) * 16 + (wid >> 1) * 4) * 1024; d.stepa = 16384; d.lda = wid * 2048;
}
template <bool KV> DI void g1_body(char* __restrict__ ws, const u16* __restrict__ x, const u16* __restrict__ wqkv, const u16* __restrict__ w1,
                                   const u16* __restrict__ w2, const u16* __restrict__ wo, int rb, int xc, int tl, u64 tag, Dma& d, QState& qs, const XKeep& xk LT_ARG) {
  constexpr int NT = KV ? 6 : 7;
  const int wid = __builtin_amdgcn_readfirstlane(threadIdx.x >> 6), wm = wid & 3, grp = wid >> 2;
  int lane = threadIdx.x & 63, g = lane >> 4, q = (lane & 15) >> 2, p = lane & 3;
  const int hp = head_pair(xc, tl);
  const int fl1 = (tl & 1) * 192, fl2 = KV ? 320 + (tl & 1) * 64 : fl1 + 64, fx = 128 + (tl & 1) * 32;
  gu64* flg = (gu64*)(ws + FLG_OFF);
  Adr ad; Pf pf;
  d.sa = make_srd(ws + XN_OFF, S_ * D_ * 2); d.va0 = d.va1 = lane * 16; d.soa = (rb * 16 + 2 * wid) * 1024; d.stepa = 131072; d.lda = wid * 2048;
  ad.a = wm * 4096 + lane * 16;
  const unsigned lp = (g & 1) * 512 + q * 64 + (g & 1) * 32 + p * 8;
  ad.l0 = 16384 + grp * 2048 + (g >> 1) * 1024 + lp; ad.l1 = ad.l0 ^ 32; ad.lx = (16384 + 12288 + (g >> 1) * 2048 + lp) ^ (grp * 32);
  g1_dma_b(d, KV, wid < 6 ? wid : 6, lane, xc, tl, wqkv, w1); d.sob += 2 * d.stepb;
  if (KV && wid >= 6) { d.sob = 0x40000000u; d.stepb = 0; }
  pf.i = rb * 4 + tl; pf.wid = wid;
  pf.wqkv = wqkv; pf.w1 = w1; pf.w2 = w2; pf.wo = wo; pf.xc = xc;
  ad = adr_at(ad, 0);
  f32x4 acc[NT][4];
#pragma unroll
  for (int t = 0; t < NT; ++t) {
    f32x4 v = {0.f, 0.f, 0.f, 0.f};
    if (t >= (KV ? 4 : 2)) v = lds_bias(G1_EXTRA + 4096 + g * 8 + (t == 6 ? fx + grp * 16 : (t < 4 ? fl1 : fl2) + grp * 32 + (t & 1) * 16) * 2);
#pragma unroll
    for (int m = 0; m < 4; ++m) { acc[t][m] = v; asm volatile("" : "+v"(acc[t][m])); }
  }
  asm volatile("" : "+v"(d.vb0), "+v"(d.vb1));
  asm volatile("" : "+s"(d.sob), "+s"(d.stepb), "+s"(d.ldb), "+s"(d.sb));
  {
    const Adr ad1 = adr_at(ad, 2 * G1_SLOT);
    Frag<NT> f0, f1;
    if (wid == 1) await_all(flg + (FL_XN + xc * 8 + rb) * 32 + (lane & 31), tag);
    bar();
    LT(4);
    k_dma_a<0>(d); k_dma_a<G1_SLOT>(d);
    k_dma_a<2 * G1_SLOT>(d); k_dma_b<2, 2 * G1_SLOT>(d); k_dma_a<3 * G1_SLOT>(d); k_dma_b<2, 3 * G1_SLOT>(d);
    wait_vm<10>(); bar();
    k_read_all<NT, 4096, 0>(f0, ad, std::make_integer_sequence<int, 4 + 2 * NT>{});
    wait_lgkm0(); wait_vm<8>();
    {
      const auto rxr = __builtin_amdgcn_make_buffer_rsrc(ws + XR_OFF, 0, S_ * D_ * 2, 0x00020000);
      __builtin_amdgcn_raw_buffer_store_b128(xk.v0, rxr, xk.off, 0, 16); __builtin_amdgcn_raw_buffer_store_b128(xk.v1, rxr, xk.off + 16 * 131072, 0, 16);
    }
    if (wid == 7) first_touches(wqkv, w1, xc, rb * 4 + tl, lane, 2 + EARLY_STEPS, G1_PF0 - 2 - EARLY_STEPS);
    LT(5); LC(20);
    g1_mainloop<NT, KV>(acc, f0, f1, ad, ad1, d, pf LT_PASS);
    lane = fresh_lane();
    asm volatile("" : "+v"(lane));
    g2_setup_b(d, w2, wid, lane);
    k_step<NT, 0, KV, 4096, G1_SLOT, 0, 0, 0>(acc, f0, f1, ad, d);
    k_step<NT, 0, KV, 4096, 0, 0, 0, 1>(acc, f1, f0, ad1, d);
    k_step<NT, 0, KV, 4096, G1_SLOT, 0, 0, 2>(acc, f0, f1, ad1, d);
    k_step<NT, 0, KV, 4096, 0, 0, 0, 3>(acc, f1, f0, ad, d);
    d.sob += 6 * d.stepb;
    __builtin_amdgcn_s_setprio(0);
    acc_fence();
  }
  LT(6); LC(21);
  lane = fresh_lane();
  asm volatile("" : "+v"(lane));
  g = lane >> 4; q = (lane & 15) >> 2; p = lane & 3;
  const int tid = wid * 64 + lane;
  const int hd = hp * 2 + grp;
  const f32x4 zero = {0.f, 0.f, 0.f, 0.f};
  const int G = ((g & 1) << 1) | (g >> 1);
  const auto ra2 = __builtin_amdgcn_make_buffer_rsrc(ws + A2_OFF, 0, (unsigned)A2_BYTES, 0x00020000);
  const unsigned a2r = (rb * KS2 * 16 + wm * 4) * 1024 + (lane & 15) * 16, a2b = a2r + xc * 14 * 16384;
  gu64* fh = flg + FL_H * 32;
  ffn_pair2(acc[4], acc[5], ra2, a2b + ((fl2 >> 5) + grp) * 16384 + G * 256);
  if constexpr (!KV) {
    ffn_pair2(acc[2], acc[3], ra2, a2b + ((fl1 >> 5) + grp) * 16384 + G * 256);
#pragma unroll
    for (int m = 0; m < 4; m += 2) {
      const u32x2 u = ffn_act(acc[NT - 1][m]), v = ffn_act(acc[NT - 1][m + 1]);
      __builtin_amdgcn_raw_buffer_store_b128(frag_natural(u[0], u[1], v[0], v[1]), ra2, a2b + (fx >> 5) * 16384 + (grp * 2 + (g >> 1)) * 256 + (m + (g & 1)) * 1024, 0, H_AUX);
    }
  }
  SB();
  bf16x8 (&pos)[4] = qs.pos; bf16x8 (&neg)[4] = qs.neg;
  f32x4 pp[4][2];
  float mx[4], nm[4];
  {
    bf16x8 wp[2];
#pragma unroll
    for (int ft = 0; ft < 2; ++ft) { const unsigned a = G1_EXTRA + grp * 2048 + (4 * g + q) * 64 + ft * 32 + p * 8; wp[ft] = cat(lds_tr(a), lds_tr(a + 1024)); }
#pragma unroll
    for (int m = 0; m < 4; ++m) { const bf16x8 qf = pack8(acc[0][m], acc[1][m]); pp[m][0] = MFMA(wp[0], qf, zero); pp[m][1] = MFMA(wp[1], qf, zero); }
    dual_max(pp, mx, nm);
  }
  const u32x4 onesu = {0x3f803f80u, 0x3f803f80u, 0x3f803f80u, 0x3f803f80u};
  const bf16x8 ones = __builtin_bit_cast(bf16x8, onesu);
  const auto rkv = __builtin_amdgcn_make_buffer_rsrc(ws + KVP_OFF, 0, H_ * 8 * KVP_STRIDE, 0x00020000);
  const int t2 = tid & 255;
  g2_setup_a(d, ws, wid, lane);
  asm volatile("" : "+v"(mx[0]), "+v"(mx[1]), "+v"(mx[2]), "+v"(mx[3]), "+v"(nm[0]), "+v"(nm[1]), "+v"(nm[2]), "+v"(nm[3]));
  wait_vm<0>();
  bar();
  if (tid < 4) post(fh + ((tid * 2 + (rb >> 2)) * 8 + rb) * 32 + xc * 4 + tl, tag);
  if constexpr (KV) {
#pragma unroll
    for (int m = 0; m < 4; ++m) { pos[m] = half_exp(pp[m], L2E, mx[m]); neg[m] = half_exp(pp[m], -L2E, nm[m]); }
    bf16x8 idm[2];
#pragma unroll
    for (int ft = 0; ft < 2; ++ft) {
      const unsigned one = (g == q) ? ((p & 1) ? 0x3f800000u : 0x00003f80u) : 0u;
      u32x4 iv = {0u, 0u, 0u, 0u};
      iv[ft * 2] = (p >> 1) == 0 ? one : 0u; iv[ft * 2 + 1] = (p >> 1) == 1 ? one : 0u;
      idm[ft] = __builtin_bit_cast(bf16x8, iv);
    }
    f32x4 kv[4][2], zz[4];
#pragma unroll
    for (int f4 = 0; f4 < 4; ++f4) { kv[f4][0] = zero; kv[f4][1] = zero; zz[f4] = zero; }
#pragma unroll
    for (int u = 0; u < 2; ++u) {
      f32x4 tr[2][4];
#pragma unroll
      for (int mm = 0; mm < 2; ++mm) {
        const int m = 2 * u + mm;
        const f32x4 sp = MFMA(pos[m], ones, zero), sn = MFMA(neg[m], ones, zero);
        f32x4 rp, rn;
#pragma unroll
        for (int r = 0; r < 4; ++r) { rp[r] = __builtin_amdgcn_rcpf(sp[r]); rn[r] = __builtin_amdgcn_rcpf(sn[r]); }
        tr[mm][0] = MFMA(pos[m], idm[0], zero) * rp; tr[mm][1] = MFMA(pos[m], idm[1], zero) * rp;
        tr[mm][2] = MFMA(neg[m], idm[0], zero) * rn; tr[mm][3] = MFMA(neg[m], idm[1], zero) * rn;
      }
      const bf16x8 v0 = pack8(acc[2][2 * u], acc[2][2 * u + 1]), v1 = pack8(acc[3][2 * u], acc[3][2 * u + 1]);
#pragma unroll
      for (int f4 = 0; f4 < 4; ++f4) {
        const bf16x8 ph = pack8(tr[0][f4], tr[1][f4]);
        kv[f4][0] = MFMA(ph, v0, kv[f4][0]); kv[f4][1] = MFMA(ph, v1, kv[f4][1]); zz[f4] = MFMA(ph, ones, zz[f4]);
      }
    }
    const unsigned red = wm * G2_SLOT + grp * 8192, redz = KVS + wid * 256;
#pragma unroll
    for (int f4 = 0; f4 < 4; ++f4) {
#pragma unroll
      for (int t = 0; t < 2; ++t) *(LAS f32x4*)(red + (f4 & 1) * 4096 + ((f4 >> 1) * 2 + t) * 1024 + lane * 16) = kv[f4][t];
      if ((lane & 15) == 0) *(LAS f32x4*)(redz + ((f4 >> 1) * 4 + g) * 32 + (f4 & 1) * 16) = zz[f4];
    }
    wait_lgkm0();
    bar();
    {
      f32x4 s0 = zero, s1 = zero;
#pragma unroll
      for (int w = 0; w < 4; ++w) { s0 += *(const LAS f32x4*)(w * G2_SLOT + grp * 8192 + t2 * 16); s1 += *(const LAS f32x4*)(w * G2_SLOT + grp * 8192 + 4096 + t2 * 16); }
      __builtin_amdgcn_raw_buffer_store_b128(__builtin_bit_cast(u32x4, pack8(s0, s1)), rkv, (hd * 8 + rb) * KVP_STRIDE + t2 * 16, 0, 16);
      if (t2 >= 248) {
        s0 = zero; s1 = zero;
#pragma unroll
        for (int w = 0; w < 4; ++w) { s0 += *(const LAS f32x4*)(KVS + (grp * 4 + w) * 256 + (t2 - 248) * 32); s1 += *(const LAS f32x4*)(KVS + (grp * 4 + w) * 256 + (t2 - 248) * 32 + 16); }
        __builtin_amdgcn_raw_buffer_store_b128(__builtin_bit_cast(u32x4, pack8(s0, s1)), rkv, (hd * 8 + rb) * KVP_STRIDE + 4096 + (t2 - 248) * 16, 0, 16);
      }
    }
    LT(7);
    wait_vm<0>(); wait_lgkm0();
    if (wid == 1) await_all(fh + (xc * 8 + g2_pos().rb) * 32 + (lane & 31), tag);
    bar();
    gu64* fk = flg + (FL_KV + hp) * 32;
    if (tid == 0) post(fk + rb, tag);
    LT(8);
    g2_first_a<0>(d); g2_first_a<1>(d); g2_first_a<2>(d); g2_first_a<3>(d); g2_first_a<4>(d); g2_first_a<5>(d);
    LT(9);
    if (wm == 3) {
      await_all(fk + (lane & 7), tag);
      __builtin_amdgcn_fence(__ATOMIC_ACQUIRE, "wavefront");
      const unsigned ch = lane < 33 ? (rb * 33 + lane) * 16u : OOB;
      f32x4 s0 = zero, s1 = zero;
      u32x4 pv[8];
#pragma unroll
      for (int b = 0; b < 8; ++b) pv[b] = __builtin_amdgcn_raw_buffer_load_b128(rkv, ch, (hd * 8 + b) * KVP_STRIDE, 16);
#pragma unroll
      for (int b = 0; b < 8; ++b) { s0 += (f32x4){lo_f(pv[b][0]), hi_f(pv[b][0]), lo_f(pv[b][1]), hi_f(pv[b][1])}; s1 += (f32x4){lo_f(pv[b][2]), hi_f(pv[b][2]), lo_f(pv[b][3]), hi_f(pv[b][3])}; }
      __builtin_amdgcn_raw_buffer_store_b128(__builtin_bit_cast(u32x4, pack8(s0, s1)), __builtin_amdgcn_make_buffer_rsrc(ws + KVT_OFF, 0, H_ * KVP_STRIDE, 0x00020000), ch, hd * KVP_STRIDE, 16);
      wait_vm<0>();
      if (lane == 0) post(flg + (FL_KT + hp) * 32 + grp * 8 + rb, tag);
    }
#pragma unroll
    for (int m = 0; m < 4; ++m) { qs.rp[m] = 0.f; qs.rn[m] = 0.f; }
  } else {
    LT(7);
#pragma unroll
    for (int m = 0; m < 2; ++m) { pos[m] = half_exp(pp[m], L2E, mx[m]); neg[m] = half_exp(pp[m], -L2E, nm[m]); asm volatile("" : "+v"(pos[m]), "+v"(neg[m])); }
    LT(8);
    if (wid == 1) await_all(fh + (xc * 8 + g2_pos().rb) * 32 + (lane & 31), tag);
    bar();
    LT(9);
    g2_first_a<0>(d); g2_first_a<1>(d);
    SB(); pos[2] = half_exp(pp[2], L2E, mx[2]); SB();
    g2_first_a<2>(d);
    SB(); neg[2] = half_exp(pp[2], -L2E, nm[2]); SB();
    g2_first_a<3>(d);
    SB(); pos[3] = half_exp(pp[3], L2E, mx[3]); SB();
    g2_first_a<4>(d);
    SB(); neg[3] = half_exp(pp[3], -L2E, nm[3]); SB();
    g2_first_a<5>(d);
#pragma unroll
    for (int m = 0; m < 4; ++m) { qs.rp[m] = __builtin_amdgcn_rcpf(MFMA(ones, pos[m], zero)[0]); qs.rn[m] = __builtin_amdgcn_rcpf(MFMA(ones, neg[m], zero)[0]); }
    asm volatile("" : "+v"(qs.rp[0]), "+v"(qs.rp[1]), "+v"(qs.rp[2]), "+v"(qs.rp[3]), "+v"(qs.rn[0]), "+v"(qs.rn[1]), "+v"(qs.rn[2]), "+v"(qs.rn[3]));
  }
}

#define O_STEP 6

#define OUT_AUX 16
DI void o_stage(char* __restrict__ ws, const QState& qs, int rb, int hp, int wid, int lane) {
  const int wm = wid & 3, grp = wid >> 2, g = lane >> 4, hd = hp * 2 + grp;
  const f32x4 zero = {0.f, 0.f, 0.f, 0.f};
  const unsigned ks = KVS + grp * 4352;
  bf16x8 kv[2][2], zf[2];
#pragma unroll
  for (int pn = 0; pn < 2; ++pn) {
    kv[pn][0] = lds_b128(ks + (pn * 2) * 1024 + lane * 16); kv[pn][1] = lds_b128(ks + (pn * 2 + 1) * 1024 + lane * 16);
    zf[pn] = lds_b128(ks + 4096 + (pn * 4 + g) * 16);
  }
  const auto ra2 = __builtin_amdgcn_make_buffer_rsrc(ws + A2_OFF, 0, (unsigned)A2_BYTES, 0x00020000);
  const unsigned ob = ((rb * KS2 + 112 + hd) * 16 + wm * 4) * 1024 + (lane & 15) * 16 + (((g & 1) << 1) | (g >> 1)) * 256;
#pragma unroll
  for (int m = 0; m < 4; ++m) {
    const f32x4 np0 = MFMA(kv[0][0], qs.pos[m], zero), np1 = MFMA(kv[0][1], qs.pos[m], zero), nn0 = MFMA(kv[1][0], qs.neg[m], zero), nn1 = MFMA(kv[1][1], qs.neg[m], zero);
    const f32x4 dp = MFMA(zf[0], qs.pos[m], zero), dn = MFMA(zf[1], qs.neg[m], zero);
    const float inv = __builtin_amdgcn_rcpf(dp[0] * qs.rp[m] + dn[0] * qs.rn[m] + 1e-6f);
    const float cp = qs.rp[m] * inv, cn = qs.rn[m] * inv;
    const f32x4 o0 = np0 * cp + nn0 * cn, o1 = np1 * cp + nn1 * cn;
    __builtin_amdgcn_raw_buffer_store_b128(frag_natural(cvtpk(o0[0], o0[1]), cvtpk(o0[2], o0[3]), cvtpk(o1[0], o1[1]), cvtpk(o1[2], o1[3])), ra2, ob + m * 1024, 0, H_AUX);
  }
  wait_lgkm0();
}
DI void peek_row(char* ws, int row, int wid, int lane) {
  const i32x4 sf = make_srd(ws + FLG_OFF + row * 256, 256);
  asm volatile("s_mov_b32 m0, %2\n\ts_nop 0\n\tbuffer_load_dword %0, %1, 0 offen sc1 lds" ::"v"(lane * 4), "s"(sf), "s"(OFL + wid * 256) : "memory");
}
DI void check_row(char* ws, int row, int word, u64 tag, int wid) {
  wait_vm<6>();
  if (!__all(*(const LAS u64*)(OFL + wid * 256 + word * 8) == tag)) await_all((gu64*)(ws + FLG_OFF) + row * 32 + word, tag);
}
DI void g2_body(char* __restrict__ ws, const u16* __restrict__ wo, const u16* __restrict__ x, const u16* __restrict__ b2, u16* __restrict__ out, u64 tag, Dma& d, const QState& qs, int my_o, int wid LT_ARG) {
  LT(10);
  int lane = fresh_lane();
  asm volatile("" : "+v"(lane));
  const int tid = wid * 64 + lane;
  const int wm = wid & 3, wn = wid >> 2;
  const G2Pos ps = g2_pos();
  const int kq = ps.kq, rb = ps.rb, ct = ps.ct;
  const int g = lane >> 4, q = (lane & 15) >> 2, p = lane & 3;
  gu64* flg = (gu64*)(ws + FLG_OFF);
  Adr ad0;
  ad0.a = wm * 4096 + lane * 16;
  ad0.l0 = 16384 + wn * 4096 + (g >> 1) * 1024 + (g & 1) * 512 + q * 64 + (g & 1) * 32 + p * 8; ad0.l1 = ad0.l0 ^ 32; ad0.lx = 0;
  ad0 = adr_at(ad0, 0);
  const int G = ((g & 1) << 1) | (g >> 1);
  const unsigned go = (rb * 256 + wm * 64 + kq * 16 + (lane & 15)) * D_ + ct * 128 + wn * 64 + G * 8;
  u32x4 xv[2], bv[2];
  f32x4 acc[4][4];
#pragma unroll
  for (int t = 0; t < 4; ++t)
#pragma unroll
    for (int m = 0; m < 4; ++m) { acc[t][m] = (f32x4){0.f, 0.f, 0.f, 0.f}; asm volatile("" : "+v"(acc[t][m])); }
  {
    const Adr ad1 = adr_at(ad0, 2 * G2_SLOT), ad2 = adr_at(ad0, 4 * G2_SLOT);
    Frag<4> f0, f1;
    wait_vm<14>();
    bar();
    k_read_all<4, 2048, 0>(f0, ad0, std::make_integer_sequence<int, 12>{});
    wait_lgkm0();
    wait_vm<12>();
    LT(11); LC(22);
    const bool isq = my_o >= 0;
    const int o_hp = my_o & 31;
#define G2S(S) k_step<4, 1, false, 2048, ((((S) + 1) % 6) & 1) * G2_SLOT, ((S) % 6) * G2_SLOT, (S) < 30 ? 4 : (S) < 34 ? (33 - (S)) * 3 + 4 : -1, (S) < 30 ? -1 : (S) < 35 ? 10 : 11>(acc, ((S) & 1) ? f1 : f0, ((S) & 1) ? f0 : f1, (((S) + 1) % 6) < 2 ? ad0 : (((S) + 1) % 6) < 4 ? ad1 : ad2, d)
    constexpr int O_AT = O_STEP;
    auto extras = [&](auto sc) {
      constexpr int S = decltype(sc)::value;
      if constexpr (S == O_AT) { if (isq) peek_row(ws, FL_KT + o_hp, wid, lane); }
      if constexpr (S == O_AT + 2) {
        if (isq) {
          check_row(ws, FL_KT + o_hp, wn * 8 + (lane & 7), tag, wid);
          const i32x4 st = make_srd(ws + KVT_OFF + (o_hp * 2 + wn) * KVP_STRIDE, KVP_STRIDE);
          set_m0<KVS>(wn * 4352 + wm * 1024); nop1(); dma_ld_dev<0>(st, lane * 16, wm * 1024);
          if (wm == 0) asm volatile("s_mov_b32 m0, %2\n\ts_nop 0\n\tbuffer_load_dword %0, %1, 0 offen sc1 lds" ::"v"(4096 + lane * 4), "s"(st), "s"(KVS + wn * 4352 + 4096) : "memory");
        }
      }
      if constexpr (S == O_AT + 4) { if (isq) { __builtin_amdgcn_s_setprio(0); wait_vm<6>(); bar(); acc_fence(); o_stage(ws, qs, my_o >> 5, o_hp, wid, lane); SB(); } }
      if constexpr (S == O_AT + 6) wait_vm<6>();
      if constexpr (S == O_AT + 7) { if (isq && tid == 0) post(flg + (FL_O + (my_o >> 5) * 4 + (o_hp >> 2)) * 32 + (o_hp & 3), tag); }
      if constexpr (S == 19) peek_row(ws, FL_O + rb * 4 + kq, wid, lane);
      if constexpr (S == 21) {
        check_row(ws, FL_O + rb * 4 + kq, lane & 3, tag, wid);
        d.soa = ((rb * KS2 + 112 + kq * 8) * 16 + (wid >> 1) * 4) * 1024;
        d.sb = make_srd(wo, (kq + 1) * 256 * 2048u); d.sob = g2_sob0(wid, ct) + kq * 256 * 2048;
      }
      if constexpr (S == 29) {
#pragma unroll
        for (int h = 0; h < 2; ++h) { xv[h] = __builtin_amdgcn_raw_buffer_load_b128(__builtin_amdgcn_make_buffer_rsrc(ws + XR_OFF, 0, S_ * D_ * 2, 0x00020000), (((ct * 4 + wn * 2 + h) * 128 + rb * 16 + wm * 4 + kq) * 64 + G * 16 + (lane & 15)) * 16, 0, 16); bv[h] = *(const u32x4*)(b2 + ct * 128 + wn * 64 + G * 8 + h * 32); }
        SB();
      }
    };
    auto run = [&](auto... sc) { ((G2S(decltype(sc)::value), extras(sc)), ...); };
    [&]<int... S>(std::integer_sequence<int, S...>) { run(std::integral_constant<int, S>{}...); }(std::make_integer_sequence<int, 36>{});
    __builtin_amdgcn_s_setprio(0);
    acc_fence();
  }
  LT(12); LC(23);
  const auto rx = __builtin_amdgcn_make_buffer_rsrc(ws + XCH_OFF, 0, 256u * 49152u, 0x00020000);
#pragma unroll
  for (int i = 1; i < 4; ++i)
#pragma unroll
    for (int h = 0; h < 2; ++h)
      __builtin_amdgcn_raw_buffer_store_b128(__builtin_bit_cast(u32x4, pack8(acc[2 * h][i], acc[2 * h + 1][i])), rx, ((blockIdx.x & ~6) | (((kq + i) & 3) << 1)) * 49152 + (((i - 1) * 2 + h) * 8 + wid) * 1024 + lane * 16, 0, 16);
  wait_vm<0>();
  bar();
  LT(13);
  if (tid < 3) post(flg + (FL_X + ((blockIdx.x & ~6) | (((kq + 1 + tid) & 3) << 1))) * 32 + tid, tag);
  if (wid == 1) await_all(flg + (FL_X + blockIdx.x) * 32 + (lane & 63) % 3, tag);
  bar();
  __builtin_amdgcn_fence(__ATOMIC_ACQUIRE, "wavefront");
  LT(14);
  const auto rout = __builtin_amdgcn_make_buffer_rsrc(out, 0, S_ * D_ * 2, 0x00020000);
  u32x4 pr[3][2];
#pragma unroll
  for (int j = 0; j < 3; ++j)
#pragma unroll
    for (int h = 0; h < 2; ++h) pr[j][h] = __builtin_amdgcn_raw_buffer_load_b128(rx, blockIdx.x * 49152 + ((j * 2 + h) * 8 + wid) * 1024 + lane * 16, 0, 16);
#pragma unroll
  for (int h = 0; h < 2; ++h) {
    f32x4 v0 = acc[2 * h][0], v1 = acc[2 * h + 1][0];
#pragma unroll
    for (int j = 0; j < 3; ++j) {
      v0 += (f32x4){lo_f(pr[j][h][0]), hi_f(pr[j][h][0]), lo_f(pr[j][h][1]), hi_f(pr[j][h][1])};
      v1 += (f32x4){lo_f(pr[j][h][2]), hi_f(pr[j][h][2]), lo_f(pr[j][h][3]), hi_f(pr[j][h][3])};
    }
#pragma unroll
    for (int r = 0; r < 4; ++r) { float a = v0[r], b = v1[r]; swap16(a, b); v0[r] = a; v1[r] = b; }
    u32x4 o;
    o[0] = cvtpk(v0[0] + lo_f(xv[h][0]) + lo_f(bv[h][0]), v0[1] + hi_f(xv[h][0]) + hi_f(bv[h][0]));
    o[1] = cvtpk(v0[2] + lo_f(xv[h][1]) + lo_f(bv[h][1]), v0[3] + hi_f(xv[h][1]) + hi_f(bv[h][1]));
    o[2] = cvtpk(v1[0] + lo_f(xv[h][2]) + lo_f(bv[h][2]), v1[1] + hi_f(xv[h][2]) + hi_f(bv[h][2]));
    o[3] = cvtpk(v1[2] + lo_f(xv[h][3]) + lo_f(bv[h][3]), v1[3] + hi_f(xv[h][3]) + hi_f(bv[h][3]));
    __builtin_amdgcn_raw_buffer_store_b128(o, rout, (unsigned)(go + h * 32) * 2, 0, OUT_AUX);
  }
  if (blockIdx.x == 0 && tid == 0) post(flg, tag);
  LT(15);
}

__global__ __launch_bounds__(512, 2) void k_all(char* __restrict__ ws, const u16* __restrict__ x, const u16* __restrict__ wqkv, const u16* __restrict__ wphi, const u16* __restrict__ wo,
                                                const u16* __restrict__ w1, const u16* __restrict__ b1, const u16* __restrict__ w2, const u16* __restrict__ b2, u16* __restrict__ out) {
  LT(0);
  const int xc = blockIdx.x & 7, idx = blockIdx.x >> 3, rb = idx >> 2, tl = idx & 3;
  XKeep xk;
  const u64 tag = start_phase(ws, x, wqkv, wphi, w1, b1, b2, rb, xc, tl, xk LT_PASS);
  Dma d; QState qs;
  if (tl < 2) g1_body<true>(ws, x, wqkv, w1, w2, wo, rb, xc, tl, tag, d, qs, xk LT_PASS);
  else g1_body<false>(ws, x, wqkv, w1, w2, wo, rb, xc, tl, tag, d, qs, xk LT_PASS);
  g2_body(ws, wo, x, b2, out, tag, d, qs, tl < 2 ? -1 : rb * 32 + head_pair(xc, tl), __builtin_amdgcn_readfirstlane(threadIdx.x >> 6) LT_PASS);
}

extern "C" void kernel_launch(void* const* d_in, const int* in_sizes, int n_in, void* d_out, int out_size, void* d_ws, size_t ws_size, hipStream_t stream) {
  const u16* x = (const u16*)d_in[0]; const u16* wqkv = (const u16*)d_in[1]; const u16* wphi = (const u16*)d_in[2]; const u16* wo = (const u16*)d_in[3];
  const u16* w1 = (const u16*)d_in[5]; const u16* b1 = (const u16*)d_in[6]; const u16* w2 = (const u16*)d_in[7]; const u16* b2 = (const u16*)d_in[8];
  char* ws = (char*)d_ws;
  constexpr int LDS = LDS_BYTES;
  (void)hipFuncSetAttribute((const void*)k_all, hipFuncAttributeMaxDynamicSharedMemorySize, LDS);
  k_all<<<256, 512, LDS, stream>>>(ws, x, wqkv, wphi, wo, w1, b1, w2, b2, (u16*)d_out);
}
```

```cpp
#include <hip/hip_runtime.h>
#include <utility>

typedef unsigned short u16;
typedef __bf16 bf16x8 __attribute__((ext_vector_type(8)));
typedef __bf16 bf16x2 __attribute__((ext_vector_type(2)));
typedef short s16x4 __attribute__((ext_vector_type(4)));
typedef float f32x2 __attribute__((ext_vector_type(2)));
typedef float f32x4 __attribute__((ext_vector_type(4)));
typedef unsigned u32x2 __attribute__((ext_vector_type(2)));
typedef unsigned u32x4 __attribute__((ext_vector_type(4)));
typedef int i32x4 __attribute__((ext_vector_type(4)));
#define LAS __attribute__((address_space(3)))
#define GAS __attribute__((address_space(1)))
#define DI __device__ __forceinline__
#define SB() __builtin_amdgcn_sched_barrier(0)
#define MFMA(a, b, c) __builtin_amdgcn_mfma_f32_16x16x32_bf16((a), (b), (c), 0, 0, 0)

constexpr int S_ = 2048, D_ = 1024, H_ = 32, F_ = 3584;
constexpr int KS2 = 144;
constexpr size_t XN_OFF = 0;
constexpr size_t A2_OFF = 4u << 20;
constexpr size_t A2_BYTES = (size_t)S_ * 4608 * 2;
constexpr size_t XR_OFF = 24u << 20;
constexpr size_t KVP_OFF = 32u << 20;
constexpr int KVP_STRIDE = 4352;
constexpr size_t XCH_OFF = 36u << 20;
constexpr size_t FLG_OFF = 52u << 20;
constexpr int FL_XN = 1, FL_H = 65, FL_KV = 129, FL_KT = 145, FL_O = 161, FL_X = 193;
constexpr size_t KVT_OFF = 34u << 20;

DI unsigned cvtpk(float lo, float hi) { f32x2 v = {lo, hi}; bf16x2 b = __builtin_convertvector(v, bf16x2); return __builtin_bit_cast(unsigned, b); }
DI float rbf(float x) { return __uint_as_float(cvtpk(x, 0.f) << 16); }
DI float lo_f(unsigned p) { return __uint_as_float(p << 16); }
DI float hi_f(unsigned p) { return __uint_as_float(p & 0xffff0000u); }
DI bf16x8 pack8(const f32x4& a, const f32x4& b) { u32x4 r = {cvtpk(a[0], a[1]), cvtpk(a[2], a[3]), cvtpk(b[0], b[1]), cvtpk(b[2], b[3])}; return __builtin_bit_cast(bf16x8, r); }
DI i32x4 make_srd(const void* p, unsigned bytes) { unsigned long long a = (unsigned long long)p; i32x4 r; r[0] = (int)(unsigned)a; r[1] = (int)((unsigned)(a >> 32) & 0xffffu); r[2] = (int)bytes; r[3] = 0x00020000; return r; }

template <int OFF> DI void dma16(const i32x4& srd, unsigned voff, unsigned soff, unsigned lds) {
  asm volatile("s_mov_b32 m0, %3\n\ts_nop 0\n\tbuffer_load_dwordx4 %0, %1, %2 offen offset:%4 lds" ::"v"(voff), "s"(srd), "s"(soff), "s"(lds), "i"(OFF) : "memory");
}
template <int IMM> DI void set_m0(unsigned base) { asm volatile("s_add_u32 m0, %0, %1" ::"s"(base), "i"(IMM) : "scc"); }
template <int OFF> DI void dma_ld(const i32x4& srd, unsigned voff, unsigned soff) {
  asm volatile("buffer_load_dwordx4 %0, %1, %2 offen offset:%3 lds" ::"v"(voff), "s"(srd), "s"(soff), "i"(OFF) : "memory");
}
template <int OFF> DI void dma_ld_dev(const i32x4& srd, unsigned voff, unsigned soff) {
  asm volatile("buffer_load_dwordx4 %0, %1, %2 offen offset:%3 sc1 lds" ::"v"(voff), "s"(srd), "s"(soff), "i"(OFF) : "memory");
}
DI void nop1() { asm volatile("s_nop 0"); }
DI void touch(const i32x4& srd, unsigned voff, unsigned soff, unsigned lds) {
  asm volatile("s_mov_b32 m0, %3\n\ts_nop 0\n\tbuffer_load_dword %0, %1, %2 offen lds" ::"v"(voff), "s"(srd), "s"(soff), "s"(lds) : "memory");
}
DI void touch_nt(const i32x4& srd, unsigned voff, unsigned soff, unsigned lds) {
  asm volatile("s_mov_b32 m0, %3\n\ts_nop 0\n\tbuffer_load_dword %0, %1, %2 offen nt lds" ::"v"(voff), "s"(srd), "s"(soff), "s"(lds) : "memory");
}
constexpr unsigned OOB = 0x7fffffffu;
template <int N> DI void wait_vm() { asm volatile("s_waitcnt vmcnt(%0)" ::"i"(N) : "memory"); }
DI void wait_lgkm0() { __builtin_amdgcn_s_waitcnt(0xC07F); }
DI void bar() { asm volatile("" ::: "memory"); __builtin_amdgcn_s_barrier(); asm volatile("" ::: "memory"); }
DI bf16x8 lds_b128(unsigned addr) { return *(const LAS bf16x8*)(addr); }
DI s16x4 lds_tr(unsigned addr) { return __builtin_amdgcn_ds_read_tr16_b64_v4i16((LAS s16x4*)(addr)); }
DI bf16x8 cat(const s16x4& a, const s16x4& b) { return __builtin_bit_cast(bf16x8, __builtin_shufflevector(a, b, 0, 1, 2, 3, 4, 5, 6, 7)); }

#define LCN(i)
#define LT_ARG
#define LT_PASS
#define LT(i)
#define LC(i)

DI int head_pair(int xc, int tl) { return ((7 * xc + (tl < 2 ? 7 : 3)) & 7) + 8 * (tl & 1); }
DI unsigned qkv_col(int xc, int c) { return (c >> 1) * 2048 + head_pair(xc, (c < 2 ? 2 : 0) + (c & 1)) * 128; }
DI unsigned qkv_lines(int xc, int i, int lane) { const int r = lane / 6, c = lane % 6; return lane < 48 ? (unsigned)((r * 32 + i) * 6144) + qkv_col(xc, c) : OOB; }
DI unsigned w1_lines(int xc, int i, int lane) { const int r = lane / 7, c = lane % 7; return lane < 56 ? (unsigned)((r * 32 + i) * 7168 + xc * 896 + c * 128) : OOB; }
typedef unsigned long long u64;
typedef GAS u64 gu64;
DI void post(gu64* w, u64 tag) { __hip_atomic_store(w, tag, __ATOMIC_RELAXED, __HIP_MEMORY_SCOPE_AGENT); }
DI void await_all(gu64* w, u64 tag) {
#pragma nounroll
  for (unsigned n = 0; n < (1u << 20); ++n) {
    if (__all(__hip_atomic_load(w, __ATOMIC_RELAXED, __HIP_MEMORY_SCOPE_AGENT) == tag)) return;
    __builtin_amdgcn_s_sleep(1);
  }
}
DI void mfma_acc(f32x4& acc, const bf16x8& a, const bf16x8& b) { asm volatile("v_mfma_f32_16x16x32_bf16 %0, %1, %2, %0" : "+v"(acc) : "v"(a), "v"(b)); }
DI void acc_fence() { SB(); asm volatile("s_nop 7\n\ts_nop 7"); SB(); }
template <int NT> struct Frag { bf16x8 a[4]; s16x4 b[NT][2]; };
struct Adr { unsigned a, l0, l1, lx; };
struct Dma { i32x4 sa, sb; unsigned va0, va1, vb0, vb1, soa, sob, stepa, stepb, lda, ldb; };
#define KTAIL 3
#define DSP1 0
#define DSP2 0
template <int NT, int LSTR, int RO, int R> DI void k_read(Frag<NT>& f, const Adr& ad) {
  if constexpr (R < 2) f.b[0][R] = lds_tr(ad.l0 + RO + R * 256);
  else if constexpr (R < 6) f.a[R - 2] = lds_b128(ad.a + RO + (R - 2) * 1024);
  else {
    constexpr int t = (R - 4) >> 1, e = (R - 4) & 1;
    if constexpr (t < 6) f.b[t][e] = lds_tr(((t & 1) ? ad.l1 : ad.l0) + RO + (t >> 1) * LSTR + e * 256);
    else f.b[t][e] = lds_tr(ad.lx + RO + e * 256);
  }
}
template <int NT, int LSTR, int RO, int... R> DI void k_read_all(Frag<NT>& f, const Adr& ad, std::integer_sequence<int, R...>) { (k_read<NT, LSTR, RO, R>(f, ad), ...); }
template <int NB, int WO, int P> DI void k_dma(Dma& d) {
  if constexpr (P == 0) dma_ld_dev<0>(d.sa, d.va0, d.soa);
  if constexpr (P == 1) { dma_ld_dev<1024>(d.sa, d.va1, d.soa); d.soa += d.stepa; if constexpr (NB > 0) set_m0<WO>(d.ldb); }
  if constexpr (P == 2) { dma_ld<0>(d.sb, d.vb0, d.sob); if constexpr (NB == 1) d.sob += d.stepb; }
  if constexpr (P == 3) { dma_ld<2048>(d.sb, d.vb1, d.sob); d.sob += d.stepb; }
}
template <int WO> DI void k_dma_a(Dma& d) { set_m0<WO>(d.lda); nop1(); k_dma<0, WO, 0>(d); k_dma<0, WO, 1>(d); }
DI void touch(const i32x4& srd, unsigned voff, unsigned soff, unsigned lds);
template <int NB, int WO> DI void k_dma_b(Dma& d) { set_m0<WO>(d.ldb); nop1(); k_dma<NB, WO, 2>(d); if constexpr (NB > 1) k_dma<NB, WO, 3>(d); }
template <int NT, int NB, int LSTR, int RO, int WO, int J> DI void k_memop(Frag<NT>& n, const Adr& ad, Dma& d) {
  constexpr int ND = 2 + NB, TOT = 4 + 2 * NT + ND, DSP = NB == 2 ? DSP1 : DSP2;
  constexpr int before = DSP > 0 ? (J / DSP < ND ? J / DSP : ND) : (J * ND) / TOT, after = DSP > 0 ? ((J + 1) / DSP < ND ? (J + 1) / DSP : ND) : ((J + 1) * ND) / TOT;
  if constexpr (after > before) k_dma<NB, WO, before>(d); else k_read<NT, LSTR, RO, J - before>(n, ad);
  if constexpr (J == 0) set_m0<WO>(d.lda);
}
constexpr int G1_SLOT = 31744, G2_SLOT = 24576;
template <int SLOT> DI void g2_b_piece(const Dma& d) { set_m0<SLOT * G2_SLOT>(d.ldb); nop1(); dma_ld<0>(d.sb, SLOT >= 4 ? d.vb1 : d.vb0, d.sob + SLOT * 32 * 2048); }
template <int NT, int NB, bool VT, int LSTR, int RO, int WO, int VM, int LAST, int I, int... J>
DI void k_gap(f32x4 (&acc)[NT][4], const Frag<NT>& c, Frag<NT>& n, const Adr& ad, Dma& d, std::integer_sequence<int, J...>) {
  constexpr int NM = 4 * NT, ND = 2 + NB, TOT = 4 + 2 * NT + (LAST < 0 ? ND : 0), SPAN = NM - KTAIL;
  constexpr int t = I >> 2, m = (t & 1) ? 3 - (I & 3) : (I & 3);
  if constexpr (VT && (t == 2 || t == 3)) mfma_acc(acc[t][m], c.a[m], cat(c.b[t][0], c.b[t][1]));
  else mfma_acc(acc[t][m], cat(c.b[t][0], c.b[t][1]), c.a[m]);
  SB();
  if constexpr (I == NM - 1) __builtin_amdgcn_s_setprio(3);
  if constexpr (I == SPAN / 3) __builtin_amdgcn_s_setprio(2);
  if constexpr (I == 2 * SPAN / 3) __builtin_amdgcn_s_setprio(1);
  if constexpr (I == SPAN) __builtin_amdgcn_s_setprio(0);
  if constexpr (LAST < 0) {
    (((J * SPAN / TOT == I) ? k_memop<NT, NB, LSTR, RO, WO, J>(n, ad, d) : (void)0), ...);
    if constexpr (I == NM - 2) { wait_lgkm0(); wait_vm<VM * ND>(); }
  } else if constexpr (LAST >= 10) {
    if constexpr (LAST == 10) (((J * SPAN / TOT == I) ? k_read<NT, LSTR, RO, J>(n, ad) : (void)0), ...);
    if constexpr (I == NM - 2) { wait_lgkm0(); if constexpr (VM >= 0) wait_vm<VM>(); }
  } else {
    if constexpr (LAST < 3) (((J * SPAN / TOT == I) ? k_read<NT, LSTR, RO, J>(n, ad) : (void)0), ...);
    if constexpr (I == 4) g2_b_piece<LAST>(d);
    if constexpr (I == 12 && LAST == 0) g2_b_piece<5>(d);
    if constexpr (I == 12 && LAST == 3) g2_b_piece<4>(d);
    if constexpr (I == NM - 2) { wait_lgkm0(); if constexpr (LAST == 0) wait_vm<8>(); if constexpr (LAST == 1) wait_vm<6>(); }
  }
  SB();
}
template <int NT, int NB, bool VT, int LSTR, int RO, int WO, int VM, int LAST, int... I>
DI void k_gaps(f32x4 (&acc)[NT][4], const Frag<NT>& c, Frag<NT>& n, const Adr& ad, Dma& d, std::integer_sequence<int, I...>) {
  (k_gap<NT, NB, VT, LSTR, RO, WO, VM, LAST, I>(acc, c, n, ad, d, std::make_integer_sequence<int, 4 + 2 * NT + (LAST < 0 ? 2 + NB : 0)>{}), ...);
}
template <int NT, int NB, bool VT, int LSTR, int RO, int WO, int VM, int LAST = -1> DI void k_step(f32x4 (&acc)[NT][4], const Frag<NT>& c, Frag<NT>& n, const Adr& ad, Dma& d) {
  bar(); SB();
  k_gaps<NT, NB, VT, LSTR, RO, WO, VM, LAST>(acc, c, n, ad, d, std::make_integer_sequence<int, 4 * NT>{});
}
DI Adr adr_at(const Adr& b, unsigned o) { Adr r = {b.a + o, b.l0 + o, b.l1 + o, b.lx + o}; asm volatile("" : "+v"(r.a), "+v"(r.l0), "+v"(r.l1), "+v"(r.lx)); return r; }
constexpr unsigned BIAS = 4096;
DI i32x4 make_srd_b(const void* p, unsigned bytes) { return make_srd((const char*)p - BIAS, bytes + BIAS); }

constexpr int G1_EXTRA = G2_SLOT * 6, G1_SCR = G1_EXTRA + 5120, KVS = G1_SCR + 768, LDS_BYTES = KVS + 2 * 4352;
constexpr int OFL = G1_EXTRA;
#define PF0 12
constexpr int G1_PF0 = PF0;
struct Pf { i32x4 s; unsigned v, o; int it; };
DI int fresh_lane() { return __builtin_amdgcn_mbcnt_hi(~0u, __builtin_amdgcn_mbcnt_lo(~0u, 0u)); }
template <int NT, bool KV> DI void g1_mainloop(f32x4 (&acc)[NT][4], Frag<NT>& f0, Frag<NT>& f1, const Adr& ad0, const Adr& ad1, Dma& d, const Pf& pf LT_ARG) {
  constexpr int NB = 2;
  for (int it = 0; it < 7; ++it) {
    k_step<NT, NB, KV, 4096, G1_SLOT, 0, 2>(acc, f0, f1, ad0, d);
    k_step<NT, NB, KV, 4096, 0, G1_SLOT, 2>(acc, f1, f0, ad1, d);
    k_step<NT, NB, KV, 4096, G1_SLOT, 2 * G1_SLOT, 2>(acc, f0, f1, ad1, d);
    k_step<NT, NB, KV, 4096, 0, 3 * G1_SLOT, 2>(acc, f1, f0, ad0, d);
    if (__builtin_expect(it == pf.it, 0)) touch(pf.s, pf.v, pf.o, G1_SCR);
  }
}

DI float max3f(float a, float b, float c) { return __builtin_fmaxf(__builtin_fmaxf(a, b), c); }
DI float max2f(float a, float b) { return __builtin_fmaxf(a, b); }
DI void swap16(float& a, float& b) { auto r = __builtin_amdgcn_permlane16_swap(__float_as_uint(a), __float_as_uint(b), false, false); a = __uint_as_float(r[0]); b = __uint_as_float(r[1]); }
DI void swap32(float& a, float& b) { auto r = __builtin_amdgcn_permlane32_swap(__float_as_uint(a), __float_as_uint(b), false, false); a = __uint_as_float(r[0]); b = __uint_as_float(r[1]); }
constexpr float L2E = 1.4426950408889634f;
DI void dual_max(const f32x4 (&p)[4][2], float (&mx)[4], float (&nm)[4]) {
#pragma unroll
  for (int m = 0; m < 4; ++m) {
    mx[m] = max2f(max3f(max3f(p[m][0][0], p[m][0][1], p[m][0][2]), p[m][0][3], p[m][1][0]), max3f(p[m][1][1], p[m][1][2], p[m][1][3]));
    nm[m] = max2f(max3f(max3f(-p[m][0][0], -p[m][0][1], -p[m][0][2]), -p[m][0][3], -p[m][1][0]), max3f(-p[m][1][1], -p[m][1][2], -p[m][1][3]));
  }
  float a[4];
#pragma unroll
  for (int m = 0; m < 4; ++m) { swap16(mx[m], nm[m]); a[m] = max2f(mx[m], nm[m]); }
  swap32(a[0], a[1]); swap32(a[2], a[3]);
  float b0 = max2f(a[0], a[1]) * -L2E, b1 = max2f(a[2], a[3]) * -L2E;
  a[0] = b0; a[1] = b0; a[2] = b1; a[3] = b1;
  swap32(a[0], a[1]); swap32(a[2], a[3]);
#pragma unroll
  for (int m = 0; m < 4; ++m) { mx[m] = a[m]; nm[m] = a[m]; swap16(mx[m], nm[m]); }
}
DI void q_tile(const f32x4 (&p)[2], float mx, float nm, bf16x8& pos, bf16x8& neg, float& eps) {
  const u32x4 onesu = {0x3f803f80u, 0x3f803f80u, 0x3f803f80u, 0x3f803f80u};
  const bf16x8 ones = __builtin_bit_cast(bf16x8, onesu);
  const f32x4 zero = {0.f, 0.f, 0.f, 0.f};
  f32x4 e0, e1, n0, n1;
#pragma unroll
  for (int r = 0; r < 4; ++r) { n0[r] = __builtin_amdgcn_exp2f(__builtin_fmaf(p[0][r], -L2E, nm)); n1[r] = __builtin_amdgcn_exp2f(__builtin_fmaf(p[1][r], -L2E, nm)); }
  const float rn = __builtin_amdgcn_rcpf(MFMA(ones, pack8(n0, n1), zero)[0]);
#pragma unroll
  for (int r = 0; r < 4; ++r) { e0[r] = __builtin_amdgcn_exp2f(__builtin_fmaf(p[0][r], L2E, mx)); e1[r] = __builtin_amdgcn_exp2f(__builtin_fmaf(p[1][r], L2E, mx)); }
  pos = pack8(e0, e1);
  const float sp = MFMA(ones, pos, zero)[0];
  neg = pack8(n0 * (sp * rn), n1 * (sp * rn));
  eps = 1e-6f * sp;
}
DI bf16x8 half_exp(const f32x4 (&p)[2], float c, float off) {
  f32x4 e0, e1;
#pragma unroll
  for (int r = 0; r < 4; ++r) { e0[r] = __builtin_amdgcn_exp2f(__builtin_fmaf(p[0][r], c, off)); e1[r] = __builtin_amdgcn_exp2f(__builtin_fmaf(p[1][r], c, off)); }
  return pack8(e0, e1);
}
DI u32x4 frag_natural(unsigned x0, unsigned x1, unsigned y0, unsigned y1) {
  auto r0 = __builtin_amdgcn_permlane16_swap(x0, y0, false, false);
  auto r1 = __builtin_amdgcn_permlane16_swap(x1, y1, false, false);
  u32x4 o = {r0[0], r1[0], r0[1], r1[1]};
  return o;
}
DI float relu(float v) { const int b = __float_as_int(v); return __int_as_float(b > 0 ? b : 0); }
DI u32x2 ffn_act(const f32x4& v) {
  u32x2 r = {cvtpk(v[0] * relu(v[0]), v[1] * relu(v[1])), cvtpk(v[2] * relu(v[2]), v[3] * relu(v[3]))};
  return r;
}
DI f32x4 lds_bias(unsigned addr) { const u32x2 v = *(const LAS u32x2*)(addr); f32x4 r = {lo_f(v[0]), hi_f(v[0]), lo_f(v[1]), hi_f(v[1])}; return r; }
#define H_AUX 16
template <class R> DI void ffn_pair2(const f32x4 (&t0)[4], const f32x4 (&t1)[4], const R& rs, unsigned off) {
#pragma unroll
  for (int m = 0; m < 4; ++m) { const u32x2 x = ffn_act(t0[m]), y = ffn_act(t1[m]); __builtin_amdgcn_raw_buffer_store_b128((u32x4){x[0], x[1], y[0], y[1]}, rs, off + m * 1024, 0, H_AUX); }
}
template <int CTRL> DI float dpp_add(float v) { return v + __uint_as_float(__builtin_amdgcn_update_dpp(0u, __float_as_uint(v), CTRL, 0xf, 0xf, false)); }
DI float wave_sum(float v) {
  v = dpp_add<0x121>(v); v = dpp_add<0x122>(v); v = dpp_add<0x124>(v); v = dpp_add<0x128>(v);
  float w = v; swap16(v, w); v += w;
  w = v; swap32(v, w); return v + w;
}
DI void g1_dma_b(Dma& d, bool kv, int wid, int lane, int xc, int tl, const u16* wqkv, const u16* w1) {
  const int line = wid >> 1, fl1 = (tl & 1) * 192, fl2 = kv ? 320 + (tl & 1) * 64 : fl1 + 64, fx = 128 + (tl & 1) * 32;
  const bool isq = line == 0 || (kv && line == 1);
  const unsigned pitch = isq ? 6144u : 7168u;
  d.sb = isq ? make_srd_b(wqkv, 1024u * 6144u) : make_srd_b(w1, 1024u * 7168u);
  const int r = lane >> 2, c = (lane & 3) ^ (((r >> 3) & 1) << 1);
  d.vb0 = BIAS + r * pitch + c * 16;
  d.vb1 = d.vb0 - 2048 + (line < 3 ? 64u : 16u * pitch);
  const int col = isq ? (kv ? line + 1 : 0) * 1024 + head_pair(xc, tl) * 64 : xc * 448 + (line == 1 ? fl1 : line == 2 ? fl2 : fx);
  d.sob = col * 2 + (wid & 1) * 16 * pitch; d.stepb = 32 * pitch; d.ldb = 16384 + line * 4096 + (wid & 1) * 1024;
}
#define XPOL " nt"
#define W1N 2
#define XSTR 4
#define HSTR 1
#define EARLY_STEPS 4
constexpr unsigned CODE_BYTES = 36u << 10;
struct XKeep { u32x4 v0, v1; unsigned off, lt_off; Adr ad, ad1; Pf pf; };
DI u64 start_phase(char* __restrict__ ws, const u16* __restrict__ x, const u16* __restrict__ wqkv, const u16* __restrict__ wphi, const u16* __restrict__ w1,
                   const u16* __restrict__ b1, const u16* __restrict__ b2, int rb, int xc, int tl, XKeep& xk, Dma& d, u64 pc0 LT_ARG) {
  const int tid = threadIdx.x, wid = __builtin_amdgcn_readfirstlane(tid >> 6), lane = tid & 63;
  gu64* flg = (gu64*)(ws + FLG_OFF);
  const int lrow = blockIdx.x * 8 + (tid & 7);
  {
    const i32x4 sx = make_srd(x, S_ * D_ * 2);
    const unsigned vo = (blockIdx.x * 8 + wid) * 2048 + lane * 16;
    set_m0<3 * G1_SLOT>(wid * 2064); nop1();
    asm volatile("buffer_load_dwordx4 %0, %1, 0 offen" XPOL " lds\n\tbuffer_load_dwordx4 %0, %1, 0 offen offset:1024" XPOL " lds" ::"v"(vo), "s"(sx) : "memory");
  }
  u64 tag = *(const __attribute__((address_space(4))) u64*)(ws + FLG_OFF);
  const bool kv = tl < 2;
  g1_dma_b(d, kv, wid < 6 ? wid : 6, lane, xc, tl, wqkv, w1);
  if (kv && wid >= 6) { d.sob = 0x40000000u; d.stepb = 0; }
  d.sob = __builtin_amdgcn_readfirstlane(d.sob); d.stepb = __builtin_amdgcn_readfirstlane(d.stepb);
  k_dma_b<2, 0>(d); k_dma_b<2, G1_SLOT>(d);
  const int iw = rb * 4 + tl;
  if (wid < 4) dma16<0>(make_srd(wphi, H_ * 2048), lane * 16, head_pair(xc, tl) * 4096 + wid * 1024, G1_EXTRA + wid * 1024);
  else if (wid == 4) dma16<0>(make_srd(b1, F_ * 2), lane * 16, xc * 896, G1_EXTRA + 4096);
  else if (wid == 5) { if (kv) touch(make_srd((const void*)pc0, CODE_BYTES), lane < 32 ? lane * 128u : OOB, (rb * 2 + tl) * 4096, G1_SCR); else touch(make_srd(b2, D_ * 2), lane < 2 ? lane * 128u : OOB, rb * 256, G1_SCR); }
  else if (wid == 6) touch(make_srd(w1, 1024u * 7168u), lane < 7 * EARLY_STEPS ? w1_lines(xc, iw, lane) : OOB, 64 * 7168, G1_SCR);
  else touch(make_srd(wqkv, 1024u * 6144u), lane < 6 * EARLY_STEPS ? qkv_lines(xc, iw, lane) : OOB, 64 * 6144, G1_SCR);
  wait_vm<5>();
  LT(1);
  {
    const u32x4 v0 = *(const LAS u32x4*)(3 * G1_SLOT + wid * 2064 + lane * 16), v1 = *(const LAS u32x4*)(3 * G1_SLOT + wid * 2064 + 1024 + lane * 16);
    f32x2 xf[8];
#pragma unroll
    for (int j = 0; j < 4; ++j) { xf[j] = (f32x2){lo_f(v0[j]), hi_f(v0[j])}; xf[4 + j] = (f32x2){lo_f(v1[j]), hi_f(v1[j])}; }
    const f32x2 sv = ((xf[0] + xf[1]) + (xf[2] + xf[3])) + ((xf[4] + xf[5]) + (xf[6] + xf[7]));
    float mean = wave_sum(sv[0] + sv[1]) * (1.f / D_);
    asm("" : "+v"(mean));
    f32x2 q0 = {0.f, 0.f}, q1 = q0;
#pragma unroll
    for (int j = 0; j < 8; j += 2) { const f32x2 a = xf[j] - mean, b = xf[j + 1] - mean; q0 += a * a; q1 += b * b; }
    q0 += q1;
    const float var = wave_sum(q0[0] + q0[1]) * (1.f / D_);
    const float mb = rbf(mean), inv = __builtin_amdgcn_rcpf(rbf(__builtin_amdgcn_sqrtf(rbf(rbf(var) + 1e-5f))));
#pragma unroll
    for (int i = 0; i < 2; ++i) {
      u32x4 o;
#pragma unroll
      for (int j = 0; j < 4; ++j) { const f32x2 c = xf[i * 4 + j] - mb; const unsigned dd = cvtpk(c[0], c[1]); o[j] = cvtpk(lo_f(dd) * inv, hi_f(dd) * inv); }
      *(LAS u32x4*)(2 * G1_SLOT + wid * 2064 + i * 1024 + lane * 16) = o;
    }
  }
  wait_lgkm0(); bar();
  const unsigned xo = ((((tid >> 3) >> 2) * 128 + (lrow >> 4)) * 64 + ((tid >> 3) & 3) * 16 + (lrow & 15)) * 16;
  const unsigned tl_ = (tid & 7) * 2064 + (tid >> 3) * 16;
  {
    const auto rxn = __builtin_amdgcn_make_buffer_rsrc(ws + XN_OFF, 0, S_ * D_ * 2, 0x00020000);
#pragma unroll
    for (int i = 0; i < 2; ++i) __builtin_amdgcn_raw_buffer_store_b128(*(const LAS u32x4*)(2 * G1_SLOT + tl_ + i * 1024), rxn, xo + i * 16 * 131072, 0, 16);
  }
  const u32x4 v0 = *(const LAS u32x4*)(3 * G1_SLOT + tl_), v1 = *(const LAS u32x4*)(3 * G1_SLOT + tl_ + 1024);
  LT(2);
  SB();
  {
    const int wm = wid & 3, grp = wid >> 2, g = lane >> 4, q = (lane & 15) >> 2, p = lane & 3;
    d.sa = make_srd(ws + XN_OFF, S_ * D_ * 2); d.va0 = d.va1 = lane * 16; d.soa = (rb * 16 + 2 * wid) * 1024; d.stepa = 131072; d.lda = wid * 2048;
    Adr ad;
    ad.a = wm * 4096 + lane * 16;
    const unsigned lp = (g & 1) * 512 + q * 64 + (g & 1) * 32 + p * 8;
    ad.l0 = 16384 + grp * 2048 + (g >> 1) * 1024 + lp; ad.l1 = ad.l0 ^ 32; ad.lx = (16384 + 12288 + (g >> 1) * 2048 + lp) ^ (grp * 32);
    xk.ad = adr_at(ad, 0); xk.ad1 = adr_at(ad, 2 * G1_SLOT);
    constexpr int NR = 32 - G1_PF0;
    static_assert(NR % 4 == 0 && NR <= 32, "");
    const int krow = G1_PF0 * 32 + iw * NR + (wid >> 1) * (NR / 4);
    Pf& pf = xk.pf;
    if (wid & 1) { pf.s = make_srd(wqkv, 1024u * 6144u); pf.v = lane < 6 * (NR / 4) ? (lane / 6) * 6144u + qkv_col(xc, lane % 6) : OOB; pf.o = krow * 6144; }
    else { pf.s = make_srd(w1, 1024u * 7168u); pf.v = lane < 7 * (NR / 4) ? (lane / 7) * 7168u + xc * 896 + (lane % 7) * 128 : OOB; pf.o = krow * 7168; }
    pf.it = (iw * NR) >> 7;
    xk.lt_off = (wid & 1) ? (lane < 6 * (G1_PF0 - 2 - EARLY_STEPS) ? qkv_lines(xc, iw, lane) : OOB) : (lane < 7 * (G1_PF0 - 2 - EARLY_STEPS) ? w1_lines(xc, iw, lane) : OOB);
    asm volatile("" : "+v"(pf.v), "+v"(xk.lt_off), "+v"(d.vb0), "+v"(d.vb1), "+v"(d.va0));
    asm volatile("" : "+s"(d.sob), "+s"(d.stepb), "+s"(d.ldb), "+s"(d.sb), "+s"(pf.s), "+s"(pf.o), "+s"(pf.it));
    d.va1 = d.va0;
  }
  tag = (tag ^ (tag >> 29)) * 0x9E3779B97F4A7C15ull + 0x632BE59BD9B4E019ull;
  asm volatile("" : "+s"(tag));
  wait_vm<0>(); wait_lgkm0(); bar();
  LT(3);
  if (tid < 8) post(flg + (FL_XN + tid * 8 + (blockIdx.x >> 5)) * 32 + (blockIdx.x & 31), tag);
  xk.v0 = v0; xk.v1 = v1; xk.off = xo;
  if (wid == 1) await_all(flg + (FL_XN + xc * 8 + rb) * 32 + (lane & 31), tag);
  bar();
  LT(4);
  k_dma_a<0>(d); k_dma_a<G1_SLOT>(d);
  k_dma_a<2 * G1_SLOT>(d); k_dma_b<2, 2 * G1_SLOT>(d);
  return tag;
}
struct QState { bf16x8 pos[4], neg[4]; float eps[4]; };
struct G2Pos { int kq, rb, ct; };
DI G2Pos g2_pos() { const int xc = blockIdx.x & 7, idx = blockIdx.x >> 3; G2Pos p = {xc >> 1, (xc & 1) * 4 + (idx & 3), idx >> 2}; return p; }
DI unsigned g2_sob0(int wid, int ct) { return (ct * 128 + (wid & 3) * 32) * 2; }
constexpr bool g2_natural(int stage) { return stage < 28 && (stage % 14 == 4 || stage % 14 == 5); }
DI void g2_setup_b(Dma& d, const u16* w2, int wid, int lane) {
  const G2Pos ps = g2_pos();
  d.sb = make_srd(w2, F_ * 2048u);
  const int r = lane >> 2, c = (lane & 3) ^ (((r >> 3) & 1) << 1), u = wid >> 2;
  d.vb0 = (16 * ((r >> 2) & 1) + 8 * u + 4 * (r >> 3) + (r & 3)) * 2048 + c * 16; d.vb1 = (16 * u + r) * 2048 + c * 16;
  d.sob = g2_sob0(wid, ps.ct) + ps.kq * 896 * 2048; d.stepb = 32 * 2048; d.ldb = 16384 + (wid & 3) * 2048 + (wid >> 2) * 1024;
}
template <int SL> DI void g2_first_a(Dma& d) { k_dma_a<SL * G2_SLOT>(d); if constexpr (SL >= 2) touch(d.sa, OOB, 0, G1_SCR); }
DI void g2_setup_a(Dma& d, char* ws, int wid, int lane) {
  const G2Pos ps = g2_pos();
  d.sa = make_srd_b(ws + A2_OFF, (ps.rb * KS2 + 112 + ps.kq * 8 + 8) * 16384);
  d.va0 = BIAS + lane * 16 + (((wid & 1) * 2 + ps.kq) & 3) * 1024; d.va1 = BIAS + lane * 16 + (((wid & 1) * 2 + 1 + ps.kq) & 3) * 1024 - 1024;
  d.soa = ((ps.rb * KS2 + ps.kq * 28) * 16 + (wid >> 1) * 4) * 1024; d.stepa = 16384; d.lda = wid * 2048;
}
template <bool KV> DI void g1_body(char* __restrict__ ws, const u16* __restrict__ x, const u16* __restrict__ wqkv, const u16* __restrict__ w1,
                                   const u16* __restrict__ w2, const u16* __restrict__ wo, int rb, int xc, int tl, u64 tag, Dma& d, QState& qs, const XKeep& xk LT_ARG) {
  constexpr int NT = KV ? 6 : 7;
  const int wid = __builtin_amdgcn_readfirstlane(threadIdx.x >> 6), wm = wid & 3, grp = wid >> 2;
  int lane = threadIdx.x & 63, g = lane >> 4, q = (lane & 15) >> 2, p = lane & 3;
  const int hp = head_pair(xc, tl);
  const int fl1 = (tl & 1) * 192, fl2 = KV ? 320 + (tl & 1) * 64 : fl1 + 64, fx = 128 + (tl & 1) * 32;
  gu64* flg = (gu64*)(ws + FLG_OFF);
  const Adr& ad = xk.ad; const Adr& ad1 = xk.ad1; const Pf& pf = xk.pf;
  f32x4 acc[NT][4];
#pragma unroll
  for (int t = 0; t < NT; ++t) {
    f32x4 v = {0.f, 0.f, 0.f, 0.f};
    if (t >= (KV ? 4 : 2)) v = lds_bias(G1_EXTRA + 4096 + g * 8 + (t == 6 ? fx + grp * 16 : (t < 4 ? fl1 : fl2) + grp * 32 + (t & 1) * 16) * 2);
#pragma unroll
    for (int m = 0; m < 4; ++m) { acc[t][m] = v; asm volatile("" : "+v"(acc[t][m])); }
  }
  {
    Frag<NT> f0, f1;
    wait_vm<6>(); bar();
    k_read_all<NT, 4096, 0>(f0, ad, std::make_integer_sequence<int, 4 + 2 * NT>{});
    k_dma_a<3 * G1_SLOT>(d); k_dma_b<2, 3 * G1_SLOT>(d);
    wait_lgkm0(); wait_vm<8>();
    if (wid == 7) touch(make_srd(wqkv, 1024u * 6144u), xk.lt_off, (2 + EARLY_STEPS) * 32 * 6144, G1_SCR);
    if (wid == 6) touch(make_srd(w1, 1024u * 7168u), xk.lt_off, (2 + EARLY_STEPS) * 32 * 7168, G1_SCR);
    LT(5); LC(20);
    g1_mainloop<NT, KV>(acc, f0, f1, ad, ad1, d, pf LT_PASS);
    {
      const auto rxr = __builtin_amdgcn_make_buffer_rsrc(ws + XR_OFF, 0, S_ * D_ * 2, 0x00020000);
      __builtin_amdgcn_raw_buffer_store_b128(xk.v0, rxr, xk.off, 0, 16); __builtin_amdgcn_raw_buffer_store_b128(xk.v1, rxr, xk.off + 16 * 131072, 0, 16);
    }
    lane = fresh_lane();
    asm volatile("" : "+v"(lane));
    g2_setup_b(d, w2, wid, lane);
    k_step<NT, 0, KV, 4096, G1_SLOT, 0, 0, 0>(acc, f0, f1, ad, d);
    {
      const int pc = blockIdx.x * 2 + wid;
      touch(make_srd(w2, F_ * 2048u), wid < W1N ? lane * 128u : OOB, ((pc & 3) * 28 + 6) * 65536 + (pc >> 2) * 8192, G1_SCR);
    }
    k_step<NT, 0, KV, 4096, 0, 0, 0, 1>(acc, f1, f0, ad1, d);
    k_step<NT, 0, KV, 4096, G1_SLOT, 0, 0, 2>(acc, f0, f1, ad1, d);
    k_step<NT, 0, KV, 4096, 0, 0, 0, 3>(acc, f1, f0, ad, d);
    d.sob += 6 * d.stepb;
    __builtin_amdgcn_s_setprio(0);
    acc_fence();
  }
  LT(6); LC(21);
  lane = fresh_lane();
  asm volatile("" : "+v"(lane));
  g = lane >> 4; q = (lane & 15) >> 2; p = lane & 3;
  const int tid = wid * 64 + lane;
  const int hd = hp * 2 + grp;
  const f32x4 zero = {0.f, 0.f, 0.f, 0.f};
  const auto ra2 = __builtin_amdgcn_make_buffer_rsrc(ws + A2_OFF, 0, (unsigned)A2_BYTES, 0x00020000);
  const unsigned a2r = (rb * KS2 * 16 + wm * 4) * 1024 + (lane & 15) * 16, a2b = a2r + xc * 14 * 16384;
  gu64* fh = flg + FL_H * 32;
  ffn_pair2(acc[4], acc[5], ra2, a2b + ((fl2 >> 5) + grp) * 16384 + g * 256);
  if constexpr (!KV) {
    ffn_pair2(acc[2], acc[3], ra2, a2b + ((fl1 >> 5) + grp) * 16384 + g * 256);
#pragma unroll
    for (int m = 0; m < 4; m += 2) {
      const u32x2 u = ffn_act(acc[NT - 1][m]), v = ffn_act(acc[NT - 1][m + 1]);
      __builtin_amdgcn_raw_buffer_store_b128(frag_natural(u[0], u[1], v[0], v[1]), ra2, a2b + (fx >> 5) * 16384 + (grp * 2 + (g >> 1)) * 256 + (m + (g & 1)) * 1024, 0, H_AUX);
    }
  }
  SB();
  bf16x8 (&pos)[4] = qs.pos; bf16x8 (&neg)[4] = qs.neg;
  f32x4 pp[4][2];
  float mx[4], nm[4];
  {
    bf16x8 wp[2];
#pragma unroll
    for (int ft = 0; ft < 2; ++ft) { const unsigned a = G1_EXTRA + grp * 2048 + (4 * g + q) * 64 + ft * 32 + p * 8; wp[ft] = cat(lds_tr(a), lds_tr(a + 1024)); }
#pragma unroll
    for (int m = 0; m < 4; ++m) { const bf16x8 qf = pack8(acc[0][m], acc[1][m]); pp[m][0] = MFMA(wp[0], qf, zero); pp[m][1] = MFMA(wp[1], qf, zero); }
    dual_max(pp, mx, nm);
  }
  const u32x4 onesu = {0x3f803f80u, 0x3f803f80u, 0x3f803f80u, 0x3f803f80u};
  const bf16x8 ones = __builtin_bit_cast(bf16x8, onesu);
  const auto rkv = __builtin_amdgcn_make_buffer_rsrc(ws + KVP_OFF, 0, H_ * 8 * KVP_STRIDE, 0x00020000);
  const int t2 = tid & 255;
  g2_setup_a(d, ws, wid, lane);
  asm volatile("" : "+v"(mx[0]), "+v"(mx[1]), "+v"(mx[2]), "+v"(mx[3]), "+v"(nm[0]), "+v"(nm[1]), "+v"(nm[2]), "+v"(nm[3]));
  wait_vm<0>();
  bar();
  if (tid < 4) post(fh + ((tid * 2 + (rb >> 2)) * 8 + rb) * 32 + xc * 4 + tl, tag);
  if constexpr (KV) {
    if (wid >= 4) {
      const int pc = ((rb * 2 + tl) * 8 + xc) * 4 + wm;
      if (pc < 192) touch(make_srd(w2, F_ * 2048u), lane * 128, ((pc & 3) * 28 + 22) * 65536 + (pc >> 2) * 8192, G1_SCR);
      else touch(make_srd(wo, 1024u * 2048u), lane * 128, (pc - 192) * 8192, G1_SCR);
    }
#pragma unroll
    for (int m = 0; m < 4; ++m) { pos[m] = half_exp(pp[m], L2E, mx[m]); neg[m] = half_exp(pp[m], -L2E, nm[m]); }
    bf16x8 idm[2];
#pragma unroll
    for (int ft = 0; ft < 2; ++ft) {
      const unsigned one = (g == q) ? ((p & 1) ? 0x3f800000u : 0x00003f80u) : 0u;
      u32x4 iv = {0u, 0u, 0u, 0u};
      iv[ft * 2] = (p >> 1) == 0 ? one : 0u; iv[ft * 2 + 1] = (p >> 1) == 1 ? one : 0u;
      idm[ft] = __builtin_bit_cast(bf16x8, iv);
    }
    f32x4 kv[4][2], zz[4];
#pragma unroll
    for (int f4 = 0; f4 < 4; ++f4) { kv[f4][0] = zero; kv[f4][1] = zero; zz[f4] = zero; }
#pragma unroll
    for (int u = 0; u < 2; ++u) {
      f32x4 tr[2][4];
#pragma unroll
      for (int mm = 0; mm < 2; ++mm) {
        const int m = 2 * u + mm;
        const f32x4 sp = MFMA(pos[m], ones, zero), sn = MFMA(neg[m], ones, zero);
        f32x4 rp, rn;
#pragma unroll
        for (int r = 0; r < 4; ++r) { rp[r] = __builtin_amdgcn_rcpf(sp[r]); rn[r] = __builtin_amdgcn_rcpf(sn[r]); }
        tr[mm][0] = MFMA(pos[m], idm[0], zero) * rp; tr[mm][1] = MFMA(pos[m], idm[1], zero) * rp;
        tr[mm][2] = MFMA(neg[m], idm[0], zero) * rn; tr[mm][3] = MFMA(neg[m], idm[1], zero) * rn;
      }
      const bf16x8 v0 = pack8(acc[2][2 * u], acc[2][2 * u + 1]), v1 = pack8(acc[3][2 * u], acc[3][2 * u + 1]);
#pragma unroll
      for (int f4 = 0; f4 < 4; ++f4) {
        const bf16x8 ph = pack8(tr[0][f4], tr[1][f4]);
        kv[f4][0] = MFMA(ph, v0, kv[f4][0]); kv[f4][1] = MFMA(ph, v1, kv[f4][1]); zz[f4] = MFMA(ph, ones, zz[f4]);
      }
    }
    const unsigned red = wm * G2_SLOT + grp * 8192, redz = KVS + wid * 256;
#pragma unroll
    for (int f4 = 0; f4 < 4; ++f4) {
#pragma unroll
      for (int t = 0; t < 2; ++t) *(LAS f32x4*)(red + (f4 & 1) * 4096 + ((f4 >> 1) * 2 + t) * 1024 + lane * 16) = kv[f4][t];
      if ((lane & 15) == 0) *(LAS f32x4*)(redz + ((f4 >> 1) * 4 + g) * 32 + (f4 & 1) * 16) = zz[f4];
    }
    wait_lgkm0();
    bar();
    {
      f32x4 s0 = zero, s1 = zero;
#pragma unroll
      for (int w = 0; w < 4; ++w) { s0 += *(const LAS f32x4*)(w * G2_SLOT + grp * 8192 + t2 * 16); s1 += *(const LAS f32x4*)(w * G2_SLOT + grp * 8192 + 4096 + t2 * 16); }
      __builtin_amdgcn_raw_buffer_store_b128(__builtin_bit_cast(u32x4, pack8(s0, s1)), rkv, (hd * 8 + rb) * KVP_STRIDE + t2 * 16, 0, 16);
      if (t2 >= 248) {
        s0 = zero; s1 = zero;
#pragma unroll
        for (int w = 0; w < 4; ++w) { s0 += *(const LAS f32x4*)(KVS + (grp * 4 + w) * 256 + (t2 - 248) * 32); s1 += *(const LAS f32x4*)(KVS + (grp * 4 + w) * 256 + (t2 - 248) * 32 + 16); }
        __builtin_amdgcn_raw_buffer_store_b128(__builtin_bit_cast(u32x4, pack8(s0, s1)), rkv, (hd * 8 + rb) * KVP_STRIDE + 4096 + (t2 - 248) * 16, 0, 16);
      }
    }
    LT(7);
    wait_vm<0>(); wait_lgkm0();
    if (wid == 1) await_all(fh + (xc * 8 + g2_pos().rb) * 32 + (lane & 31), tag);
    bar();
    gu64* fk = flg + (FL_KV + hp) * 32;
    if (tid == 0) post(fk + rb, tag);
    LT(8);
    g2_first_a<0>(d); g2_first_a<1>(d); g2_first_a<2>(d); g2_first_a<3>(d); g2_first_a<4>(d); g2_first_a<5>(d);
    LT(9);
#pragma unroll
    for (int m = 0; m < 4; ++m) qs.eps[m] = 0.f;
  } else {
    LT(7);
#pragma unroll
    for (int m = 0; m < 2; ++m) { q_tile(pp[m], mx[m], nm[m], pos[m], neg[m], qs.eps[m]); asm volatile("" : "+v"(pos[m]), "+v"(neg[m]), "+v"(qs.eps[m])); }
    LT(8);
    if (wid == 1) await_all(fh + (xc * 8 + g2_pos().rb) * 32 + (lane & 31), tag);
    bar();
    LT(9);
    g2_first_a<0>(d); g2_first_a<1>(d);
    SB(); q_tile(pp[2], mx[2], nm[2], pos[2], neg[2], qs.eps[2]); SB();
    g2_first_a<2>(d); g2_first_a<3>(d);
    SB(); q_tile(pp[3], mx[3], nm[3], pos[3], neg[3], qs.eps[3]); SB();
    g2_first_a<4>(d); g2_first_a<5>(d);
    asm volatile("" : "+v"(pos[2]), "+v"(neg[2]), "+v"(qs.eps[2]), "+v"(pos[3]), "+v"(neg[3]), "+v"(qs.eps[3]));
  }
}

#define O_PARTS 1
#define SH_AT 0
#define O_STEP 8

#define OUT_AUX 16
template <int M0, int M1> DI void o_stage(char* __restrict__ ws, const QState& qs, int rb, int hp, int wid, int lane) {
  const int wm = wid & 3, grp = wid >> 2, g = lane >> 4, hd = hp * 2 + grp;
  const f32x4 zero = {0.f, 0.f, 0.f, 0.f};
  const unsigned ks = KVS + grp * 4352;
  bf16x8 kv[2][2], zf[2];
#pragma unroll
  for (int pn = 0; pn < 2; ++pn) {
    kv[pn][0] = lds_b128(ks + (pn * 2) * 1024 + lane * 16); kv[pn][1] = lds_b128(ks + (pn * 2 + 1) * 1024 + lane * 16);
    zf[pn] = lds_b128(ks + 4096 + (pn * 4 + g) * 16);
  }
  const auto ra2 = __builtin_amdgcn_make_buffer_rsrc(ws + A2_OFF, 0, (unsigned)A2_BYTES, 0x00020000);
  const unsigned ob = ((rb * KS2 + 112 + hd) * 16 + wm * 4) * 1024 + lane * 16;
#pragma unroll
  for (int m = M0; m < M1; ++m) {
    const f32x4 n0 = MFMA(kv[1][0], qs.neg[m], MFMA(kv[0][0], qs.pos[m], zero)), n1 = MFMA(kv[1][1], qs.neg[m], MFMA(kv[0][1], qs.pos[m], zero));
    const float inv = __builtin_amdgcn_rcpf(MFMA(zf[1], qs.neg[m], MFMA(zf[0], qs.pos[m], zero))[0] + qs.eps[m]);
    __builtin_amdgcn_raw_buffer_store_b128(__builtin_bit_cast(u32x4, pack8(n0 * inv, n1 * inv)), ra2, ob + m * 1024, 0, H_AUX);
  }
  wait_lgkm0();
}
DI void peek_row(char* ws, int row, int wid, int lane) {
  const i32x4 sf = make_srd(ws + FLG_OFF + row * 256, 256);
  asm volatile("s_mov_b32 m0, %2\n\ts_nop 0\n\tbuffer_load_dword %0, %1, 0 offen sc1 lds" ::"v"(lane * 4), "s"(sf), "s"(OFL + wid * 256) : "memory");
}
DI void check_row(char* ws, int row, int word, u64 tag, int wid) {
  wait_vm<6>();
  if (!__all(*(const LAS u64*)(OFL + wid * 256 + word * 8) == tag)) await_all((gu64*)(ws + FLG_OFF) + row * 32 + word, tag);
}
DI void g2_body(char* __restrict__ ws, const u16* __restrict__ wo, const u16* __restrict__ x, const u16* __restrict__ b2, u16* __restrict__ out, u64 tag, Dma& d, const QState& qs, bool isq, int my_o, int wid LT_ARG) {
  LT(10);
  int lane = fresh_lane();
  asm volatile("" : "+v"(lane));
  const int tid = wid * 64 + lane;
  const int wm = wid & 3, wn = wid >> 2;
  const G2Pos ps = g2_pos();
  const int kq = ps.kq, rb = ps.rb, ct = ps.ct;
  const int g = lane >> 4, q = (lane & 15) >> 2, p = lane & 3;
  gu64* flg = (gu64*)(ws + FLG_OFF);
  Adr ad0;
  ad0.a = wm * 4096 + lane * 16;
  ad0.l0 = 16384 + wn * 4096 + (g >> 1) * 1024 + (g & 1) * 512 + q * 64 + (g & 1) * 32 + p * 8; ad0.l1 = ad0.l0 ^ 32; ad0.lx = 0;
  ad0 = adr_at(ad0, 0);
  const int G = ((g & 1) << 1) | (g >> 1);
  const unsigned go = (rb * 256 + wm * 64 + kq * 16 + (lane & 15)) * D_ + ct * 128 + wn * 64 + G * 8;
  f32x4 acc[4][4];
#pragma unroll
  for (int t = 0; t < 4; ++t)
#pragma unroll
    for (int m = 0; m < 4; ++m) { acc[t][m] = (f32x4){0.f, 0.f, 0.f, 0.f}; asm volatile("" : "+v"(acc[t][m])); }
  {
    const Adr ad1 = adr_at(ad0, 2 * G2_SLOT), ad2 = adr_at(ad0, 4 * G2_SLOT);
    Frag<4> f0, f1;
    wait_vm<14>();
    bar();
    k_read_all<4, 2048, 0>(f0, ad0, std::make_integer_sequence<int, 12>{});
    wait_lgkm0();
    wait_vm<12>();
    LT(11); LC(22);
    const bool hw = wid == 3 || wid == 4;
    const int o_hp = my_o & 31, o_rb = my_o >> 5;
    constexpr int OS = O_STEP + 5;
    auto step = [&](auto sc) {
      constexpr int S = decltype(sc)::value, rs = (S + 1) % 6;
      bar(); SB();
      if constexpr (S == SH_AT) { if (!isq && hw) peek_row(ws, FL_KV + o_hp, wid, lane); }
      if constexpr (S == SH_AT + 2) {
        if (!isq && hw) {
          check_row(ws, FL_KV + o_hp, lane & 7, tag, wid);
          const i32x4 sp = make_srd(ws + KVP_OFF + (o_hp * 2 + wn) * 8 * KVP_STRIDE, 8 * KVP_STRIDE);
          const unsigned vo = (lane >> 5) * KVP_STRIDE + (o_rb * 32 + (lane & 31)) * 16;
#pragma unroll
          for (int j = 0; j < 4; ++j) { set_m0<KVS>(wn * 4352 + j * 1024); nop1(); dma_ld_dev<0>(sp, vo, j * 2 * KVP_STRIDE); }
          asm volatile("s_mov_b32 m0, %2\n\ts_nop 0\n\tbuffer_load_dword %0, %1, 0 offen sc1 lds" ::"v"(lane < 32 ? (lane >> 2) * KVP_STRIDE + 4096u + o_rb * 16 + (lane & 3) * 4 : OOB), "s"(sp), "s"(KVS + wn * 4352 + 4096) : "memory");
        }
      }
      if constexpr (S == SH_AT + 4) {
        if (!isq && hw) {
          wait_vm<6>();
          f32x4 sv = {0.f, 0.f, 0.f, 0.f};
          f32x2 sz = {0.f, 0.f};
#pragma unroll
          for (int b = 0; b < 8; ++b) {
            const u32x2 v = *(const LAS u32x2*)(KVS + wn * 4352 + b * 512 + lane * 8);
            const unsigned z = *(const LAS unsigned*)(KVS + wn * 4352 + 4096 + b * 16 + (lane & 3) * 4);
            sv += (f32x4){lo_f(v[0]), hi_f(v[0]), lo_f(v[1]), hi_f(v[1])}; sz += (f32x2){lo_f(z), hi_f(z)};
          }
          const auto rt = __builtin_amdgcn_make_buffer_rsrc(ws + KVT_OFF, 0, H_ * KVP_STRIDE, 0x00020000);
          __builtin_amdgcn_raw_buffer_store_b64((u32x2){cvtpk(sv[0], sv[1]), cvtpk(sv[2], sv[3])}, rt, o_rb * 512 + lane * 8, (o_hp * 2 + wn) * KVP_STRIDE, 16);
          __builtin_amdgcn_raw_buffer_store_b32(cvtpk(sz[0], sz[1]), rt, lane < 4 ? 4096u + o_rb * 16 + lane * 4 : OOB, (o_hp * 2 + wn) * KVP_STRIDE, 16);
        }
      }
      if constexpr (S == SH_AT + 6) { if (!isq && hw) { wait_vm<6>(); if (lane == 0) post(flg + (FL_KT + o_hp) * 32 + wn * 8 + o_rb, tag); } }
      if constexpr (S == O_STEP) { if (isq && hw) peek_row(ws, FL_KT + o_hp, wid, lane); }
      if constexpr (S == O_STEP + 2) {
        if (isq && hw) {
          check_row(ws, FL_KT + o_hp, wn * 8 + (lane & 7), tag, wid);
          const i32x4 st = make_srd(ws + KVT_OFF + (o_hp * 2 + wn) * KVP_STRIDE, KVP_STRIDE);
          set_m0<KVS>(wn * 4352); nop1(); dma_ld_dev<0>(st, lane * 16, 0); dma_ld_dev<1024>(st, lane * 16, 0); dma_ld_dev<2048>(st, lane * 16, 0); dma_ld_dev<3072>(st, lane * 16, 0);
          asm volatile("s_mov_b32 m0, %2\n\ts_nop 0\n\tbuffer_load_dword %0, %1, 0 offen sc1 lds" ::"v"(4096 + lane * 4), "s"(st), "s"(KVS + wn * 4352 + 4096) : "memory");
        }
      }
      if constexpr (S == O_STEP + 4) { if (hw) wait_vm<6>(); }
      if constexpr (S == OS) { if (isq) o_stage<0, 4>(ws, qs, o_rb, o_hp, wid, lane); }
      if constexpr (S == OS + O_PARTS + 1) wait_vm<6>();
      if constexpr (S == OS + O_PARTS + 2) { if (isq && tid == 0) post(flg + (FL_O + o_rb * 4 + (o_hp >> 2)) * 32 + (o_hp & 3), tag); }
      if constexpr (S == 19) { if (wid == 2) peek_row(ws, FL_O + rb * 4 + kq, wid, lane); }
      if constexpr (S == 21) { if (wid == 2) check_row(ws, FL_O + rb * 4 + kq, lane & 3, tag, wid); }
      if constexpr (S == 22) {
        d.soa = ((rb * KS2 + 112 + kq * 8) * 16 + (wid >> 1) * 4) * 1024;
        d.sb = make_srd(wo, 1024u * 2048u); d.sob = g2_sob0(wid, ct) + kq * 256 * 2048;
      }
      if constexpr (S == 30) {
        d.sa = make_srd(ws + XR_OFF, S_ * D_ * 2); d.va0 = lane * 16; d.va1 = lane * 16 + 131072 - 1024; d.soa = ((ct * 4 + wn * 2) * 128 + rb * 16 + wm * 4 + kq) * 1024;
        d.sb = make_srd(b2 + ct * 128, 256); d.vb0 = lane * 16; d.sob = 0;
      }
      if constexpr (g2_natural(S + 6) != g2_natural(S + 5) && S > 0) { const unsigned t_ = d.vb0; d.vb0 = d.vb1; d.vb1 = t_; }
      SB();
      k_gaps<4, 1, false, 2048, (rs & 1) * G2_SLOT, (S % 6) * G2_SLOT, S < 31 ? 4 : S < 34 ? (34 - S) * 3 : -1, S < 31 ? -1 : S < 35 ? 10 : 11>(
          acc, (S & 1) ? f1 : f0, (S & 1) ? f0 : f1, rs < 2 ? ad0 : rs < 4 ? ad1 : ad2, d, std::make_integer_sequence<int, 16>{});
    };
    auto run = [&](auto... sc) { (step(sc), ...); };
    [&]<int... S>(std::integer_sequence<int, S...>) { run(std::integral_constant<int, S>{}...); }(std::make_integer_sequence<int, 36>{});
    __builtin_amdgcn_s_setprio(0);
    acc_fence();
  }
  LT(12); LC(23);
  const auto rx = __builtin_amdgcn_make_buffer_rsrc(ws + XCH_OFF, 0, 256u * 49152u, 0x00020000);
#pragma unroll
  for (int i = 1; i < 4; ++i)
#pragma unroll
    for (int h = 0; h < 2; ++h)
      __builtin_amdgcn_raw_buffer_store_b128(__builtin_bit_cast(u32x4, pack8(acc[2 * h][i], acc[2 * h + 1][i])), rx, ((blockIdx.x & ~6) | (((kq + i) & 3) << 1)) * 49152 + (((i - 1) * 2 + h) * 8 + wid) * 1024 + lane * 16, 0, 16);
  wait_vm<0>();
  LT(13);
  if (lane < 3) post(flg + FL_X * 32 + ((((blockIdx.x & ~6) | (((kq + 1 + lane) & 3) << 1)) * 8 + wid) * 4 + lane) * XSTR, tag);
  u32x4 xv[2], bv[2];
#pragma unroll
  for (int h = 0; h < 2; ++h) { xv[h] = *(const LAS u32x4*)(d.lda + h * 1024 + (G * 16 + (lane & 15)) * 16); bv[h] = *(const LAS u32x4*)(d.ldb + (wn * 64 + G * 8 + h * 32) * 2); }
  await_all(flg + FL_X * 32 + ((blockIdx.x * 8 + wid) * 4 + lane % 3) * XSTR, tag);
  __builtin_amdgcn_fence(__ATOMIC_ACQUIRE, "wavefront");
  LT(14);
  const auto rout = __builtin_amdgcn_make_buffer_rsrc(out, 0, S_ * D_ * 2, 0x00020000);
  u32x4 pr[3][2];
#pragma unroll
  for (int j = 0; j < 3; ++j)
#pragma unroll
    for (int h = 0; h < 2; ++h) pr[j][h] = __builtin_amdgcn_raw_buffer_load_b128(rx, blockIdx.x * 49152 + ((j * 2 + h) * 8 + wid) * 1024 + lane * 16, 0, 16);
#pragma unroll
  for (int h = 0; h < 2; ++h) {
    f32x4 v0 = acc[2 * h][0], v1 = acc[2 * h + 1][0];
#pragma unroll
    for (int j = 0; j < 3; ++j) {
      v0 += (f32x4){lo_f(pr[j][h][0]), hi_f(pr[j][h][0]), lo_f(pr[j][h][1]), hi_f(pr[j][h][1])};
      v1 += (f32x4){lo_f(pr[j][h][2]), hi_f(pr[j][h][2]), lo_f(pr[j][h][3]), hi_f(pr[j][h][3])};
    }
#pragma unroll
    for (int r = 0; r < 4; ++r) { float a = v0[r], b = v1[r]; swap16(a, b); v0[r] = a; v1[r] = b; }
    u32x4 o;
    o[0] = cvtpk(v0[0] + lo_f(xv[h][0]) + lo_f(bv[h][0]), v0[1] + hi_f(xv[h][0]) + hi_f(bv[h][0]));
    o[1] = cvtpk(v0[2] + lo_f(xv[h][1]) + lo_f(bv[h][1]), v0[3] + hi_f(xv[h][1]) + hi_f(bv[h][1]));
    o[2] = cvtpk(v1[0] + lo_f(xv[h][2]) + lo_f(bv[h][2]), v1[1] + hi_f(xv[h][2]) + hi_f(bv[h][2]));
    o[3] = cvtpk(v1[2] + lo_f(xv[h][3]) + lo_f(bv[h][3]), v1[3] + hi_f(xv[h][3]) + hi_f(bv[h][3]));
    __builtin_amdgcn_raw_buffer_store_b128(o, rout, (unsigned)(go + h * 32) * 2, 0, OUT_AUX);
  }
  if (blockIdx.x == 0 && tid == 0) post(flg, tag);
  LT(15);
}

__global__ __launch_bounds__(512, 2) void k_all(char* __restrict__ ws, const u16* __restrict__ x, const u16* __restrict__ wqkv, const u16* __restrict__ wphi, const u16* __restrict__ wo,
                                                const u16* __restrict__ w1, const u16* __restrict__ b1, const u16* __restrict__ w2, const u16* __restrict__ b2, u16* __restrict__ out) {
  LT(0);
  u64 pc0;
  asm volatile("s_getpc_b64 %0" : "=s"(pc0));
  const int xc = blockIdx.x & 7, idx = blockIdx.x >> 3, rb = idx >> 2, tl = idx & 3;
  XKeep xk; Dma d; QState qs;
  const u64 tag = start_phase(ws, x, wqkv, wphi, w1, b1, b2, rb, xc, tl, xk, d, pc0 LT_PASS);
  if (tl < 2) g1_body<true>(ws, x, wqkv, w1, w2, wo, rb, xc, tl, tag, d, qs, xk LT_PASS);
  else g1_body<false>(ws, x, wqkv, w1, w2, wo, rb, xc, tl, tag, d, qs, xk LT_PASS);
  g2_body(ws, wo, x, b2, out, tag, d, qs, tl >= 2, rb * 32 + head_pair(xc, tl), __builtin_amdgcn_readfirstlane(threadIdx.x >> 6) LT_PASS);
}

extern "C" void kernel_launch(void* const* d_in, const int* in_sizes, int n_in, void* d_out, int out_size, void* d_ws, size_t ws_size, hipStream_t stream) {
  const u16* x = (const u16*)d_in[0]; const u16* wqkv = (const u16*)d_in[1]; const u16* wphi = (const u16*)d_in[2]; const u16* wo = (const u16*)d_in[3];
  const u16* w1 = (const u16*)d_in[5]; const u16* b1 = (const u16*)d_in[6]; const u16* w2 = (const u16*)d_in[7]; const u16* b2 = (const u16*)d_in[8];
  char* ws = (char*)d_ws;
  constexpr int LDS = LDS_BYTES;
  (void)hipFuncSetAttribute((const void*)k_all, hipFuncAttributeMaxDynamicSharedMemorySize, LDS);
  k_all<<<256, 512, LDS, stream>>>(ws, x, wqkv, wphi, wo, w1, b1, w2, b2, (u16*)d_out);
}
```

```cpp
#include <hip/hip_runtime.h>
#include <type_traits>
#include <utility>

typedef unsigned short u16;
typedef __bf16 bf16x8 __attribute__((ext_vector_type(8)));
typedef __bf16 bf16x2 __attribute__((ext_vector_type(2)));
typedef short s16x4 __attribute__((ext_vector_type(4)));
typedef float f32x2 __attribute__((ext_vector_type(2)));
typedef float f32x4 __attribute__((ext_vector_type(4)));
typedef unsigned u32x2 __attribute__((ext_vector_type(2)));
typedef unsigned u32x4 __attribute__((ext_vector_type(4)));
typedef int i32x4 __attribute__((ext_vector_type(4)));
#define LAS __attribute__((address_space(3)))
#define GAS __attribute__((address_space(1)))
#define DI __device__ __forceinline__
#define SB() __builtin_amdgcn_sched_barrier(0)
#define MFMA(a, b, c) __builtin_amdgcn_mfma_f32_16x16x32_bf16((a), (b), (c), 0, 0, 0)

constexpr int S_ = 2048, D_ = 1024, H_ = 32, F_ = 3584;
constexpr int KTAIL = 3;
constexpr int EARLY_STEPS = 4, G1_PF0 = 12;
constexpr int LA = 4;
constexpr int SH_AT = 2, O_STEP = 11, HOP = 1, G2_LATE = 31;
constexpr int SC1 = 16;
constexpr int KS2 = 144;
constexpr size_t XN_OFF = 0;
constexpr size_t A2_OFF = 4u << 20;
constexpr size_t A2_BYTES = (size_t)S_ * 4608 * 2;
constexpr size_t XR_OFF = 24u << 20;
constexpr size_t KVP_OFF = 32u << 20;
constexpr int KVP_STRIDE = 4352;
constexpr size_t XCH_OFF = 36u << 20;
constexpr size_t FLG_OFF = 52u << 20;
constexpr int FL_XN = 1, FL_H = 65, FL_KV = 129, FL_KT = 145, FL_O = 161, FL_X = 193;
constexpr size_t KVT_OFF = 34u << 20;

DI unsigned cvtpk(float lo, float hi) { f32x2 v = {lo, hi}; bf16x2 b = __builtin_convertvector(v, bf16x2); return __builtin_bit_cast(unsigned, b); }
DI float rbf(float x) { return __uint_as_float(cvtpk(x, 0.f) << 16); }
DI float lo_f(unsigned p) { return __uint_as_float(p << 16); }
DI float hi_f(unsigned p) { return __uint_as_float(p & 0xffff0000u); }
DI f32x4 rbf4(const f32x4& v) { const unsigned a = cvtpk(v[0], v[1]), b = cvtpk(v[2], v[3]); f32x4 r = {lo_f(a), hi_f(a), lo_f(b), hi_f(b)}; return r; }
DI bf16x8 pack8(const f32x4& a, const f32x4& b) { u32x4 r = {cvtpk(a[0], a[1]), cvtpk(a[2], a[3]), cvtpk(b[0], b[1]), cvtpk(b[2], b[3])}; return __builtin_bit_cast(bf16x8, r); }
DI i32x4 make_srd(const void* p, unsigned bytes) { unsigned long long a = (unsigned long long)p; i32x4 r; r[0] = (int)(unsigned)a; r[1] = (int)((unsigned)(a >> 32) & 0xffffu); r[2] = (int)bytes; r[3] = 0x00020000; return r; }

template <int OFF> DI void dma16(const i32x4& srd, unsigned voff, unsigned soff, unsigned lds) {
  asm volatile("s_mov_b32 m0, %3\n\ts_nop 0\n\tbuffer_load_dwordx4 %0, %1, %2 offen offset:%4 lds" ::"v"(voff), "s"(srd), "s"(soff), "s"(lds), "i"(OFF) : "memory");
}
template <int IMM> DI void set_m0(unsigned base) { asm volatile("s_add_u32 m0, %0, %1" ::"s"(base), "i"(IMM) : "scc"); }
template <int OFF> DI void dma_ld(const i32x4& srd, unsigned voff, unsigned soff) {
  asm volatile("buffer_load_dwordx4 %0, %1, %2 offen offset:%3 lds" ::"v"(voff), "s"(srd), "s"(soff), "i"(OFF) : "memory");
}
template <int OFF> DI void dma_ld_dev(const i32x4& srd, unsigned voff, unsigned soff) {
  asm volatile("buffer_load_dwordx4 %0, %1, %2 offen offset:%3 sc1 lds" ::"v"(voff), "s"(srd), "s"(soff), "i"(OFF) : "memory");
}
DI void nop1() { asm volatile("s_nop 0"); }
DI void touch(const i32x4& srd, unsigned voff, unsigned soff, unsigned lds) {
  asm volatile("s_mov_b32 m0, %3\n\ts_nop 0\n\tbuffer_load_dword %0, %1, %2 offen lds" ::"v"(voff), "s"(srd), "s"(soff), "s"(lds) : "memory");
}
DI void touch_dev(const i32x4& srd, unsigned voff, unsigned soff, unsigned lds) {
  asm volatile("s_mov_b32 m0, %3\n\ts_nop 0\n\tbuffer_load_dword %0, %1, %2 offen sc1 lds" ::"v"(voff), "s"(srd), "s"(soff), "s"(lds) : "memory");
}
constexpr unsigned OOB = 0x7fffffffu;
template <int N> DI void wait_vm() { asm volatile("s_waitcnt vmcnt(%0)" ::"i"(N) : "memory"); }
DI void wait_lgkm0() { __builtin_amdgcn_s_waitcnt(0xC07F); }
DI void bar() { asm volatile("" ::: "memory"); __builtin_amdgcn_s_barrier(); asm volatile("" ::: "memory"); }
DI bf16x8 lds_b128(unsigned addr) { return *(const LAS bf16x8*)(addr); }
DI s16x4 lds_tr(unsigned addr) { return __builtin_amdgcn_ds_read_tr16_b64_v4i16((LAS s16x4*)(addr)); }
DI bf16x8 cat(const s16x4& a, const s16x4& b) { return __builtin_bit_cast(bf16x8, __builtin_shufflevector(a, b, 0, 1, 2, 3, 4, 5, 6, 7)); }


DI int head_pair(int xc, int tl) { return ((7 * xc + (tl < 2 ? 7 : 3)) & 7) + 8 * (tl & 1); }
DI unsigned qkv_col(int xc, int c) { return (c >> 1) * 2048 + head_pair(xc, (c < 2 ? 2 : 0) + (c & 1)) * 128; }
DI unsigned qkv_lines(int xc, int i, int lane) { const int r = lane / 6, c = lane % 6; return lane < 48 ? (unsigned)((r * 32 + i) * 6144) + qkv_col(xc, c) : OOB; }
DI unsigned w1_lines(int xc, int i, int lane) { const int r = lane / 7, c = lane % 7; return lane < 56 ? (unsigned)((r * 32 + i) * 7168 + xc * 896 + c * 128) : OOB; }
typedef unsigned long long u64;
typedef GAS u64 gu64;
DI void post(gu64* w, u64 tag) { __hip_atomic_store(w, tag, __ATOMIC_RELAXED, __HIP_MEMORY_SCOPE_AGENT); }
DI void await_all(gu64* w, u64 tag) {
#pragma nounroll
  for (unsigned n = 0; n < (1u << 20); ++n) {
    if (__all(__hip_atomic_load(w, __ATOMIC_RELAXED, __HIP_MEMORY_SCOPE_AGENT) == tag)) return;
    __builtin_amdgcn_s_sleep(1);
  }
}
DI void mfma_acc(f32x4& acc, const bf16x8& a, const bf16x8& b) { asm volatile("v_mfma_f32_16x16x32_bf16 %0, %1, %2, %0" : "+v"(acc) : "v"(a), "v"(b)); }
DI void mfma_new(f32x4& acc, const bf16x8& a, const bf16x8& b) { asm volatile("v_mfma_f32_16x16x32_bf16 %0, %1, %2, 0" : "=&v"(acc) : "v"(a), "v"(b)); }
DI void acc_fence() { SB(); asm volatile("s_nop 7\n\ts_nop 7"); SB(); }
template <int NT> struct Frag { bf16x8 a[4]; s16x4 b[NT][2]; };
struct Adr { unsigned a, l0, l1, lx; };
struct Dma { i32x4 sa, sb; unsigned va0, va1, vb0, vb1, soa, sob, stepa, stepb, lda, ldb; };
template <int NT, int LSTR, int RO, int R> DI void k_read(Frag<NT>& f, const Adr& ad) {
  if constexpr (R < 2) f.b[0][R] = lds_tr(ad.l0 + RO + R * 256);
  else if constexpr (R < 6) f.a[R - 2] = lds_b128(ad.a + RO + (R - 2) * 1024);
  else {
    constexpr int t = (R - 4) >> 1, e = (R - 4) & 1;
    if constexpr (t < 6) f.b[t][e] = lds_tr(((t & 1) ? ad.l1 : ad.l0) + RO + (t >> 1) * LSTR + e * 256);
    else f.b[t][e] = lds_tr(ad.lx + RO + e * 256);
  }
}
template <int NT, int LSTR, int RO, int... R> DI void k_read_all(Frag<NT>& f, const Adr& ad, std::integer_sequence<int, R...>) { (k_read<NT, LSTR, RO, R>(f, ad), ...); }
template <int NB, int WO, int P> DI void k_dma(Dma& d) {
  if constexpr (P == 0) dma_ld_dev<0>(d.sa, d.va0, d.soa);
  if constexpr (P == 1) { dma_ld_dev<1024>(d.sa, d.va1, d.soa); d.soa += d.stepa; if constexpr (NB > 0) set_m0<WO>(d.ldb); }
  if constexpr (P == 2) { dma_ld<0>(d.sb, d.vb0, d.sob); if constexpr (NB == 1) d.sob += d.stepb; }
  if constexpr (P == 3) { dma_ld<2048>(d.sb, d.vb1, d.sob); d.sob += d.stepb; }
}
template <int WO> DI void k_dma_a(Dma& d) { set_m0<WO>(d.lda); nop1(); k_dma<0, WO, 0>(d); k_dma<0, WO, 1>(d); }
template <int NB, int WO> DI void k_dma_b(Dma& d) { set_m0<WO>(d.ldb); nop1(); k_dma<NB, WO, 2>(d); if constexpr (NB > 1) k_dma<NB, WO, 3>(d); }
template <int NT, int NB, int LSTR, int RO, int WO, int J> DI void k_memop(Frag<NT>& n, const Adr& ad, Dma& d) {
  constexpr int ND = 2 + NB, TOT = 4 + 2 * NT + ND, before = (J * ND) / TOT, after = ((J + 1) * ND) / TOT;
  if constexpr (after > before) k_dma<NB, WO, before>(d); else k_read<NT, LSTR, RO, J - before>(n, ad);
  if constexpr (J == 0) set_m0<WO>(d.lda);
}
constexpr int G1_SLOT = 31744, G2_SLOT = 24576;
template <int SLOT> DI void g2_b_piece(const Dma& d) { set_m0<SLOT * G2_SLOT>(d.ldb); nop1(); dma_ld<0>(d.sb, SLOT >= 4 ? d.vb1 : d.vb0, d.sob + SLOT * 32 * 2048); }
struct NoHook { template <class T> DI void operator()(T) const {} };
template <int NT, int NB, bool VT, int LSTR, int RO, int WO, int VM, int LAST, int I, class H, int... J>
DI void k_gap(f32x4 (&acc)[NT][4], const Frag<NT>& c, Frag<NT>& n, const Adr& ad, Dma& d, const H& hook, std::integer_sequence<int, J...>) {
  constexpr int NM = 4 * NT, ND = 2 + NB, TOT = 4 + 2 * NT + (LAST < 0 ? ND : 0), SPAN = NM - KTAIL;
  constexpr int t = I >> 2, m = (t & 1) ? 3 - (I & 3) : (I & 3);
  if constexpr (VT && (t == 2 || t == 3)) mfma_acc(acc[t][m], c.a[m], cat(c.b[t][0], c.b[t][1]));
  else mfma_acc(acc[t][m], cat(c.b[t][0], c.b[t][1]), c.a[m]);
  SB();
  if constexpr (I == NM - 1) __builtin_amdgcn_s_setprio(3);
  if constexpr (I == SPAN / 3) __builtin_amdgcn_s_setprio(2);
  if constexpr (I == 2 * SPAN / 3) __builtin_amdgcn_s_setprio(1);
  if constexpr (I == SPAN) __builtin_amdgcn_s_setprio(0);
  if constexpr (LAST < 0) {
    (((J * SPAN / TOT == I) ? k_memop<NT, NB, LSTR, RO, WO, J>(n, ad, d) : (void)0), ...);
    if constexpr (I == NM - 2) { wait_lgkm0(); wait_vm<VM * ND>(); }
  } else {
    if constexpr (LAST < 3) (((J * SPAN / TOT == I) ? k_read<NT, LSTR, RO, J>(n, ad) : (void)0), ...);
    hook(std::integral_constant<int, I>{});
    if constexpr (I == 4) g2_b_piece<LAST>(d);
    if constexpr (I == 12 && LAST == 0) g2_b_piece<5>(d);
    if constexpr (I == 12 && LAST == 3) g2_b_piece<4>(d);
    if constexpr (I == NM - 2) { wait_lgkm0(); if constexpr (LAST == 0) wait_vm<9>(); if constexpr (LAST == 1) wait_vm<6>(); }
  }
  SB();
}
template <int NT, int NB, bool VT, int LSTR, int RO, int WO, int VM, int LAST, class H, int... I>
DI void k_gaps(f32x4 (&acc)[NT][4], const Frag<NT>& c, Frag<NT>& n, const Adr& ad, Dma& d, const H& hook, std::integer_sequence<int, I...>) {
  (k_gap<NT, NB, VT, LSTR, RO, WO, VM, LAST, I>(acc, c, n, ad, d, hook, std::make_integer_sequence<int, 4 + 2 * NT + (LAST < 0 ? 2 + NB : 0)>{}), ...);
}
template <int NT, int NB, bool VT, int LSTR, int RO, int WO, int VM, int LAST = -1, class H = NoHook> DI void k_step(f32x4 (&acc)[NT][4], const Frag<NT>& c, Frag<NT>& n, const Adr& ad, Dma& d, const H& hook = H()) {
  bar(); SB();
  k_gaps<NT, NB, VT, LSTR, RO, WO, VM, LAST>(acc, c, n, ad, d, hook, std::make_integer_sequence<int, 4 * NT>{});
}
DI Adr adr_at(const Adr& b, unsigned o) { Adr r = {b.a + o, b.l0 + o, b.l1 + o, b.lx + o}; asm volatile("" : "+v"(r.a), "+v"(r.l0), "+v"(r.l1), "+v"(r.lx)); return r; }
constexpr unsigned BIAS = 4096;
DI i32x4 make_srd_b(const void* p, unsigned bytes) { return make_srd((const char*)p - BIAS, bytes + BIAS); }

constexpr int G1_EXTRA = G2_SLOT * 6, G1_SCR = G1_EXTRA + 5120, KVS = G1_SCR + 256, LDS_BYTES = KVS + 2 * 4352;
constexpr int OFL = G1_EXTRA;
struct Pf { i32x4 s; unsigned v, o; int it; };
DI int fresh_lane() { return __builtin_amdgcn_mbcnt_hi(~0u, __builtin_amdgcn_mbcnt_lo(~0u, 0u)); }
template <int NT, bool KV> DI void g1_mainloop(f32x4 (&acc)[NT][4], Frag<NT>& f0, Frag<NT>& f1, const Adr& ad0, const Adr& ad1, Dma& d, const Pf& pf) {
  constexpr int NB = 2;
  for (int it = 0; it < 7; ++it) {
    k_step<NT, NB, KV, 4096, G1_SLOT, 0, 2>(acc, f0, f1, ad0, d);
    k_step<NT, NB, KV, 4096, 0, G1_SLOT, 2>(acc, f1, f0, ad1, d);
    k_step<NT, NB, KV, 4096, G1_SLOT, 2 * G1_SLOT, 2>(acc, f0, f1, ad1, d);
    k_step<NT, NB, KV, 4096, 0, 3 * G1_SLOT, 2>(acc, f1, f0, ad0, d);
    if (__builtin_expect(it == pf.it, 0)) touch(pf.s, pf.v, pf.o, G1_SCR);
  }
}

DI float max3f(float a, float b, float c) { return __builtin_fmaxf(__builtin_fmaxf(a, b), c); }
DI float max2f(float a, float b) { return __builtin_fmaxf(a, b); }
DI void swap16(float& a, float& b) { auto r = __builtin_amdgcn_permlane16_swap(__float_as_uint(a), __float_as_uint(b), false, false); a = __uint_as_float(r[0]); b = __uint_as_float(r[1]); }
DI void swap32(float& a, float& b) { auto r = __builtin_amdgcn_permlane32_swap(__float_as_uint(a), __float_as_uint(b), false, false); a = __uint_as_float(r[0]); b = __uint_as_float(r[1]); }
constexpr float L2E = 1.4426950408889634f;
DI void dual_max(const f32x4 (&p)[4][2], float (&mx)[4], float (&nm)[4]) {
#pragma unroll
  for (int m = 0; m < 4; ++m) {
    mx[m] = max2f(max3f(max3f(p[m][0][0], p[m][0][1], p[m][0][2]), p[m][0][3], p[m][1][0]), max3f(p[m][1][1], p[m][1][2], p[m][1][3]));
    nm[m] = max2f(max3f(max3f(-p[m][0][0], -p[m][0][1], -p[m][0][2]), -p[m][0][3], -p[m][1][0]), max3f(-p[m][1][1], -p[m][1][2], -p[m][1][3]));
  }
  float a[4];
#pragma unroll
  for (int m = 0; m < 4; ++m) { swap16(mx[m], nm[m]); a[m] = max2f(mx[m], nm[m]); }
  swap32(a[0], a[1]); swap32(a[2], a[3]);
  float b0 = max2f(a[0], a[1]) * -L2E, b1 = max2f(a[2], a[3]) * -L2E;
  a[0] = b0; a[1] = b0; a[2] = b1; a[3] = b1;
  swap32(a[0], a[1]); swap32(a[2], a[3]);
#pragma unroll
  for (int m = 0; m < 4; ++m) { mx[m] = a[m]; nm[m] = a[m]; swap16(mx[m], nm[m]); }
}
DI void q_tile(const f32x4 (&p)[2], float mx, float nm, bf16x8& pos, bf16x8& neg, float& eps) {
  const u32x4 onesu = {0x3f803f80u, 0x3f803f80u, 0x3f803f80u, 0x3f803f80u};
  const bf16x8 ones = __builtin_bit_cast(bf16x8, onesu);
  const f32x4 zero = {0.f, 0.f, 0.f, 0.f};
  f32x4 e0, e1, n0, n1;
#pragma unroll
  for (int r = 0; r < 4; ++r) { n0[r] = __builtin_amdgcn_exp2f(__builtin_fmaf(p[0][r], -L2E, nm)); n1[r] = __builtin_amdgcn_exp2f(__builtin_fmaf(p[1][r], -L2E, nm)); }
  const float rn = __builtin_amdgcn_rcpf(MFMA(ones, pack8(n0, n1), zero)[0]);
#pragma unroll
  for (int r = 0; r < 4; ++r) { e0[r] = __builtin_amdgcn_exp2f(__builtin_fmaf(p[0][r], L2E, mx)); e1[r] = __builtin_amdgcn_exp2f(__builtin_fmaf(p[1][r], L2E, mx)); }
  pos = pack8(e0, e1);
  const float sp = MFMA(ones, pos, zero)[0];
  neg = pack8(n0 * (sp * rn), n1 * (sp * rn));
  eps = 1e-6f * sp;
}
DI bf16x8 half_exp(const f32x4 (&p)[2], float c, float off) {
  f32x4 e0, e1;
#pragma unroll
  for (int r = 0; r < 4; ++r) { e0[r] = __builtin_amdgcn_exp2f(__builtin_fmaf(p[0][r], c, off)); e1[r] = __builtin_amdgcn_exp2f(__builtin_fmaf(p[1][r], c, off)); }
  return pack8(e0, e1);
}
DI u32x4 frag_natural(unsigned x0, unsigned x1, unsigned y0, unsigned y1) {
  auto r0 = __builtin_amdgcn_permlane16_swap(x0, y0, false, false);
  auto r1 = __builtin_amdgcn_permlane16_swap(x1, y1, false, false);
  u32x4 o = {r0[0], r1[0], r0[1], r1[1]};
  return o;
}
DI float relu(float v) { const int b = __float_as_int(v); return __int_as_float(b > 0 ? b : 0); }
DI u32x2 ffn_act(const f32x4& v) {
  const f32x4 t = {relu(v[0]), relu(v[1]), relu(v[2]), relu(v[3])}, u = t * t;
  u32x2 r = {cvtpk(u[0], u[1]), cvtpk(u[2], u[3])};
  return r;
}
DI f32x4 lds_bias(unsigned addr) { const u32x2 v = *(const LAS u32x2*)(addr); f32x4 r = {lo_f(v[0]), hi_f(v[0]), lo_f(v[1]), hi_f(v[1])}; return r; }
template <class R> DI void ffn_pair2(const f32x4 (&t0)[4], const f32x4 (&t1)[4], const R& rs, unsigned off) {
#pragma unroll
  for (int m = 0; m < 4; ++m) { const u32x2 x = ffn_act(t0[m]), y = ffn_act(t1[m]); __builtin_amdgcn_raw_buffer_store_b128((u32x4){x[0], x[1], y[0], y[1]}, rs, off + m * 1024, 0, SC1); }
}
template <int CTRL> DI float dpp_add(float v) { return v + __uint_as_float(__builtin_amdgcn_update_dpp(0u, __float_as_uint(v), CTRL, 0xf, 0xf, false)); }
DI float wave_sum(float v) {
  v = dpp_add<0x121>(v); v = dpp_add<0x122>(v); v = dpp_add<0x124>(v); v = dpp_add<0x128>(v);
  float w = v; swap16(v, w); v += w;
  w = v; swap32(v, w); return v + w;
}
DI void g1_dma_b(Dma& d, bool kv, int wid, int lane, int xc, int tl, const u16* wqkv, const u16* w1) {
  const int line = wid >> 1, fl1 = (tl & 1) * 192, fl2 = kv ? 320 + (tl & 1) * 64 : fl1 + 64, fx = 128 + (tl & 1) * 32;
  const bool isq = line == 0 || (kv && line == 1);
  const unsigned pitch = isq ? 6144u : 7168u;
  d.sb = isq ? make_srd_b(wqkv, 1024u * 6144u) : make_srd_b(w1, 1024u * 7168u);
  const int r = lane >> 2, c = (lane & 3) ^ (((r >> 3) & 1) << 1);
  d.vb0 = BIAS + r * pitch + c * 16;
  d.vb1 = d.vb0 - 2048 + (line < 3 ? 64u : 16u * pitch);
  const int col = isq ? (kv ? line + 1 : 0) * 1024 + head_pair(xc, tl) * 64 : xc * 448 + (line == 1 ? fl1 : line == 2 ? fl2 : fx);
  d.sob = col * 2 + (wid & 1) * 16 * pitch; d.stepb = 32 * pitch; d.ldb = 16384 + line * 4096 + (wid & 1) * 1024;
}
constexpr unsigned CODE_BYTES = 40448;
struct G2Pos { int kq, rb, ct; };
DI G2Pos g2_pos() { const int xc = blockIdx.x & 7, idx = blockIdx.x >> 3; G2Pos p = {xc >> 1, (xc & 1) * 4 + (idx & 3), idx >> 2}; return p; }
struct XKeep { u32x4 v0, v1; unsigned off, lt_off; Adr ad, ad1; Pf pf; };
DI u64 start_phase(char* __restrict__ ws, const u16* __restrict__ x, const u16* __restrict__ wqkv, const u16* __restrict__ wphi, const u16* __restrict__ w1,
                   const u16* __restrict__ b1, const u16* __restrict__ b2, int rb, int xc, int tl, XKeep& xk, Dma& d, u64 pc0) {
  const int tid = threadIdx.x, wid = __builtin_amdgcn_readfirstlane(tid >> 6), lane = tid & 63;
  gu64* flg = (gu64*)(ws + FLG_OFF);
  const int lrow = blockIdx.x * 8 + (tid & 7);
  {
    const i32x4 sx = make_srd(x, S_ * D_ * 2);
    const unsigned vo = (blockIdx.x * 8 + wid) * 2048 + lane * 16;
    set_m0<3 * G1_SLOT>(wid * 2064); nop1();
    asm volatile("buffer_load_dwordx4 %0, %1, 0 offen nt lds\n\tbuffer_load_dwordx4 %0, %1, 0 offen offset:1024 nt lds" ::"v"(vo), "s"(sx) : "memory");
  }
  u64 tag = *(const __attribute__((address_space(4))) u64*)(ws + FLG_OFF);
  const bool kv = tl < 2;
  g1_dma_b(d, kv, wid < 6 ? wid : 6, lane, xc, tl, wqkv, w1);
  if (kv && wid >= 6) { d.sob = 0x40000000u; d.stepb = 0; }
  d.sob = __builtin_amdgcn_readfirstlane(d.sob); d.stepb = __builtin_amdgcn_readfirstlane(d.stepb);
  k_dma_b<2, 0>(d); k_dma_b<2, G1_SLOT>(d);
  const int iw = rb * 4 + tl;
  if (wid < 4) dma16<0>(make_srd(wphi, H_ * 2048), lane * 16, head_pair(xc, tl) * 4096 + wid * 1024, G1_EXTRA + wid * 1024);
  else if (wid == 4) dma16<0>(make_srd(b1, F_ * 2), lane * 16, xc * 896, G1_EXTRA + 4096);
  else if (wid == 5) { if (kv) touch(make_srd((const void*)pc0, CODE_BYTES), lane < 32 ? lane * 128u : OOB, (rb * 2 + tl) * 4096, G1_SCR); else touch_dev(make_srd(tl == 2 ? (const char*)b2 + rb * 256 : ws + FLG_OFF + (FL_XN + xc * 8 + rb) * 256, 256), lane * 128, 0, G1_SCR); }
  else if (wid == 6) touch(make_srd(w1, 1024u * 7168u), lane < 7 * EARLY_STEPS ? w1_lines(xc, iw, lane) : OOB, 64 * 7168, G1_SCR);
  else touch(make_srd(wqkv, 1024u * 6144u), lane < 6 * EARLY_STEPS ? qkv_lines(xc, iw, lane) : OOB, 64 * 6144, G1_SCR);
  wait_vm<5>();
  {
    const u32x4 v0 = *(const LAS u32x4*)(3 * G1_SLOT + wid * 2064 + lane * 16), v1 = *(const LAS u32x4*)(3 * G1_SLOT + wid * 2064 + 1024 + lane * 16);
    f32x2 xf[8];
#pragma unroll
    for (int j = 0; j < 4; ++j) { xf[j] = (f32x2){lo_f(v0[j]), hi_f(v0[j])}; xf[4 + j] = (f32x2){lo_f(v1[j]), hi_f(v1[j])}; }
    const f32x2 sv = ((xf[0] + xf[1]) + (xf[2] + xf[3])) + ((xf[4] + xf[5]) + (xf[6] + xf[7]));
    float mean = wave_sum(sv[0] + sv[1]) * (1.f / D_);
    asm("" : "+v"(mean));
    f32x2 q0 = {0.f, 0.f}, q1 = q0;
#pragma unroll
    for (int j = 0; j < 8; j += 2) { const f32x2 a = xf[j] - mean, b = xf[j + 1] - mean; q0 += a * a; q1 += b * b; }
    q0 += q1;
    const float var = wave_sum(q0[0] + q0[1]) * (1.f / D_);
    const float mb = rbf(mean), inv = __builtin_amdgcn_rcpf(rbf(__builtin_amdgcn_sqrtf(rbf(rbf(var) + 1e-5f))));
#pragma unroll
    for (int i = 0; i < 2; ++i) {
      u32x4 o;
#pragma unroll
      for (int j = 0; j < 4; ++j) { const f32x2 c = xf[i * 4 + j] - mb; const unsigned dd = cvtpk(c[0], c[1]); o[j] = cvtpk(lo_f(dd) * inv, hi_f(dd) * inv); }
      *(LAS u32x4*)(2 * G1_SLOT + wid * 2064 + i * 1024 + lane * 16) = o;
    }
  }
  wait_lgkm0(); bar();
  const unsigned xo = ((((tid >> 3) >> 2) * 128 + (lrow >> 4)) * 64 + ((tid >> 3) & 3) * 16 + (lrow & 15)) * 16;
  const unsigned tl_ = (tid & 7) * 2064 + (tid >> 3) * 16;
  {
    const auto rxn = __builtin_amdgcn_make_buffer_rsrc(ws + XN_OFF, 0, S_ * D_ * 2, 0x00020000);
#pragma unroll
    for (int i = 0; i < 2; ++i) __builtin_amdgcn_raw_buffer_store_b128(*(const LAS u32x4*)(2 * G1_SLOT + tl_ + i * 1024), rxn, xo + i * 16 * 131072, 0, SC1);
  }
  const u32x4 v0 = *(const LAS u32x4*)(3 * G1_SLOT + tl_), v1 = *(const LAS u32x4*)(3 * G1_SLOT + tl_ + 1024);
  SB();
  tag = (tag ^ (tag >> 29)) * 0x9E3779B97F4A7C15ull + 0x632BE59BD9B4E019ull;
  asm volatile("" : "+s"(tag));
  wait_vm<0>();
  wait_lgkm0(); bar();
  if (tid < 8) post(flg + (FL_XN + tid * 8 + (blockIdx.x >> 5)) * 32 + (blockIdx.x & 31), tag);
  xk.v0 = v0; xk.v1 = v1; xk.off = xo;
  SB();
  {
    const int wm = wid & 3, grp = wid >> 2, g = lane >> 4, q = (lane & 15) >> 2, p = lane & 3;
    d.sa = make_srd(ws + XN_OFF, S_ * D_ * 2); d.va0 = d.va1 = lane * 16; d.soa = (rb * 16 + 2 * wid) * 1024; d.stepa = 131072; d.lda = wid * 2048;
    Adr ad;
    ad.a = wm * 4096 + lane * 16;
    const unsigned lp = (g & 1) * 512 + q * 64 + (g & 1) * 32 + p * 8;
    ad.l0 = 16384 + grp * 2048 + (g >> 1) * 1024 + lp; ad.l1 = ad.l0 ^ 32; ad.lx = (16384 + 12288 + (g >> 1) * 2048 + lp) ^ (grp * 32);
    xk.ad = adr_at(ad, 0); xk.ad1 = adr_at(ad, 2 * G1_SLOT);
    constexpr int NR = 32 - G1_PF0;
    static_assert(NR % 4 == 0 && NR <= 32, "");
    const int krow = G1_PF0 * 32 + iw * NR + (wid >> 1) * (NR / 4);
    Pf& pf = xk.pf;
    if (wid & 1) { pf.s = make_srd(wqkv, 1024u * 6144u); pf.v = lane < 6 * (NR / 4) ? (lane / 6) * 6144u + qkv_col(xc, lane % 6) : OOB; pf.o = krow * 6144; }
    else { pf.s = make_srd(w1, 1024u * 7168u); pf.v = lane < 7 * (NR / 4) ? (lane / 7) * 7168u + xc * 896 + (lane % 7) * 128 : OOB; pf.o = krow * 7168; }
    pf.it = (iw * NR) >> 7;
    xk.lt_off = (wid & 1) ? (lane < 6 * (G1_PF0 - 2 - EARLY_STEPS) ? qkv_lines(xc, iw, lane) : OOB) : (lane < 7 * (G1_PF0 - 2 - EARLY_STEPS) ? w1_lines(xc, iw, lane) : OOB);
    if (wid == 5) {
      const G2Pos ps = g2_pos();
      const int li = lane >> 1;
      const unsigned row = li == 0 ? FL_H + xc * 8 + ps.rb : li == 1 ? (kv ? FL_KV : FL_KT) + head_pair(xc, tl) : li == 2 ? FL_O + ps.rb * 4 + ps.kq : FL_X + blockIdx.x * 4 + li - 3;
      xk.lt_off = lane < 14 ? row * 256 + (lane & 1) * 128 : OOB;
    }
    asm volatile("" : "+v"(pf.v), "+v"(xk.lt_off), "+v"(d.vb0), "+v"(d.vb1), "+v"(d.va0));
    asm volatile("" : "+s"(d.sob), "+s"(d.stepb), "+s"(d.ldb), "+s"(d.sb), "+s"(pf.s), "+s"(pf.o), "+s"(pf.it));
    d.va1 = d.va0;
  }
  return tag;
}
struct QState { bf16x8 pos[4], neg[4]; float eps[4]; };
DI unsigned g2_sob0(int wid, int ct) { return (ct * 128 + (wid & 3) * 32) * 2; }
constexpr bool g2_natural(int stage) { return stage < 28 && (stage % 14 == 4 || stage % 14 == 5); }
DI void g2_setup_b(Dma& d, const u16* w2, int wid, int lane) {
  const G2Pos ps = g2_pos();
  d.sb = make_srd(w2, F_ * 2048u);
  const int r = lane >> 2, c = (lane & 3) ^ (((r >> 3) & 1) << 1), u = wid >> 2;
  d.vb0 = (16 * ((r >> 2) & 1) + 8 * u + 4 * (r >> 3) + (r & 3)) * 2048 + c * 16; d.vb1 = (16 * u + r) * 2048 + c * 16;
  d.sob = g2_sob0(wid, ps.ct) + ps.kq * 896 * 2048; d.stepb = 32 * 2048; d.ldb = 16384 + (wid & 3) * 2048 + (wid >> 2) * 1024;
}
template <int SL> DI void g2_first_a(Dma& d) { if constexpr (SL < LA) k_dma_a<SL * G2_SLOT>(d); }
DI void g2_setup_a(Dma& d, char* ws, int wid, int lane) {
  const G2Pos ps = g2_pos();
  d.sa = make_srd_b(ws + A2_OFF, (ps.rb * KS2 + 112 + ps.kq * 8 + 8) * 16384);
  d.va0 = BIAS + lane * 16 + (((wid & 1) * 2 + ps.kq) & 3) * 1024; d.va1 = BIAS + lane * 16 + (((wid & 1) * 2 + 1 + ps.kq) & 3) * 1024 - 1024;
  d.soa = ((ps.rb * KS2 + ps.kq * 28) * 16 + (wid >> 1) * 4) * 1024; d.stepa = 16384; d.lda = wid * 2048;
}
template <bool KV> DI void g1_body(char* __restrict__ ws, const u16* __restrict__ wqkv, const u16* __restrict__ w1,
                                   const u16* __restrict__ w2, const u16* __restrict__ wo, int rb, int xc, int tl, u64 tag, Dma& d, QState& qs, const XKeep& xk) {
  constexpr int NT = KV ? 6 : 7;
  const int wid = __builtin_amdgcn_readfirstlane(threadIdx.x >> 6), wm = wid & 3, grp = wid >> 2;
  int lane = threadIdx.x & 63, g = lane >> 4, q = (lane & 15) >> 2, p = lane & 3;
  const int hp = head_pair(xc, tl);
  const int fl1 = (tl & 1) * 192, fl2 = KV ? 320 + (tl & 1) * 64 : fl1 + 64, fx = 128 + (tl & 1) * 32;
  gu64* flg = (gu64*)(ws + FLG_OFF);
  const Adr& ad = xk.ad; const Adr& ad1 = xk.ad1; const Pf& pf = xk.pf;
  f32x4 acc[NT][4];
#pragma unroll
  for (int t = 0; t < NT; ++t) {
    f32x4 v = {0.f, 0.f, 0.f, 0.f};
    if (t >= (KV ? 4 : 2)) v = lds_bias(G1_EXTRA + 4096 + g * 8 + (t == 6 ? fx + grp * 16 : (t < 4 ? fl1 : fl2) + grp * 32 + (t & 1) * 16) * 2);
#pragma unroll
    for (int m = 0; m < 4; ++m) { acc[t][m] = v; asm volatile("" : "+v"(acc[t][m])); }
  }
  Dma d2;
  g2_setup_b(d2, w2, wid, lane);
  { const int pc = blockIdx.x * 2 + wid; d2.va0 = wid < 2 ? lane * 128u : OOB; d2.soa = ((pc & 3) * 28 + 6) * 65536 + (pc >> 2) * 8192; }
  asm volatile("" : "+v"(d2.vb0), "+v"(d2.vb1), "+v"(d2.va0));
  asm volatile("" : "+s"(d2.sb), "+s"(d2.sob), "+s"(d2.ldb), "+s"(d2.soa));
  {
    Frag<NT> f0, f1;
    if (wid == 1) await_all(flg + (FL_XN + xc * 8 + rb) * 32 + (lane & 31), tag);
    bar();
    k_dma_a<0>(d); k_dma_a<G1_SLOT>(d);
    k_dma_a<2 * G1_SLOT>(d); k_dma_b<2, 2 * G1_SLOT>(d);
    wait_vm<6>(); bar();
    k_read_all<NT, 4096, 0>(f0, ad, std::make_integer_sequence<int, 4 + 2 * NT>{});
    k_dma_a<3 * G1_SLOT>(d); k_dma_b<2, 3 * G1_SLOT>(d);
    wait_lgkm0(); wait_vm<8>();
    if (wid == 5) touch_dev(make_srd(ws + FLG_OFF, (FL_X + 1024) * 256), xk.lt_off, 0, G1_SCR);
    if (wid == 7) touch(make_srd(wqkv, 1024u * 6144u), xk.lt_off, (2 + EARLY_STEPS) * 32 * 6144, G1_SCR);
    if (wid == 6) touch(make_srd(w1, 1024u * 7168u), xk.lt_off, (2 + EARLY_STEPS) * 32 * 7168, G1_SCR);
    g1_mainloop<NT, KV>(acc, f0, f1, ad, ad1, d, pf);
    d.sb = d2.sb; d.vb0 = d2.vb0; d.vb1 = d2.vb1; d.sob = d2.sob; d.stepb = d2.stepb; d.ldb = d2.ldb;
    const auto rxr = __builtin_amdgcn_make_buffer_rsrc(ws + XR_OFF, 0, S_ * D_ * 2, 0x00020000);
    k_step<NT, 0, KV, 4096, G1_SLOT, 0, 0, 0>(acc, f0, f1, ad, d, [&](auto ic) {
      constexpr int I = decltype(ic)::value;
      if constexpr (I == 7) __builtin_amdgcn_raw_buffer_store_b128(xk.v0, rxr, xk.off, 0, SC1);
      if constexpr (I == 9) __builtin_amdgcn_raw_buffer_store_b128(xk.v1, rxr, xk.off, 16 * 131072, SC1);
      if constexpr (I == 16) touch(d.sb, d2.va0, d2.soa, G1_SCR);
    });
    k_step<NT, 0, KV, 4096, 0, 0, 0, 1>(acc, f1, f0, ad1, d);
    k_step<NT, 0, KV, 4096, G1_SLOT, 0, 0, 2>(acc, f0, f1, ad1, d);
    k_step<NT, 0, KV, 4096, 0, 0, 0, 3>(acc, f1, f0, ad, d);
    d.sob += 6 * d.stepb;
    __builtin_amdgcn_s_setprio(0);
    acc_fence();
  }
  lane = fresh_lane();
  asm volatile("" : "+v"(lane));
  g = lane >> 4; q = (lane & 15) >> 2; p = lane & 3;
  const int tid = wid * 64 + lane;
  const int hd = hp * 2 + grp;
  const f32x4 zero = {0.f, 0.f, 0.f, 0.f};
  const auto ra2 = __builtin_amdgcn_make_buffer_rsrc(ws + A2_OFF, 0, (unsigned)A2_BYTES, 0x00020000);
  const unsigned a2r = (rb * KS2 * 16 + wm * 4) * 1024 + (lane & 15) * 16, a2b = a2r + xc * 14 * 16384;
  gu64* fh = flg + FL_H * 32;
  ffn_pair2(acc[4], acc[5], ra2, a2b + ((fl2 >> 5) + grp) * 16384 + g * 256);
  if constexpr (!KV) {
    ffn_pair2(acc[2], acc[3], ra2, a2b + ((fl1 >> 5) + grp) * 16384 + g * 256);
#pragma unroll
    for (int m = 0; m < 4; m += 2) {
      const u32x2 u = ffn_act(acc[NT - 1][m]), v = ffn_act(acc[NT - 1][m + 1]);
      __builtin_amdgcn_raw_buffer_store_b128(frag_natural(u[0], u[1], v[0], v[1]), ra2, a2b + (fx >> 5) * 16384 + (grp * 2 + (g >> 1)) * 256 + (m + (g & 1)) * 1024, 0, SC1);
    }
  }
  SB();
  bf16x8 (&pos)[4] = qs.pos; bf16x8 (&neg)[4] = qs.neg;
  f32x4 pp[4][2];
  float mx[4], nm[4];
  {
    bf16x8 wp[2];
#pragma unroll
    for (int ft = 0; ft < 2; ++ft) { const unsigned a = G1_EXTRA + grp * 2048 + (4 * g + q) * 64 + ft * 32 + p * 8; wp[ft] = cat(lds_tr(a), lds_tr(a + 1024)); }
#pragma unroll
    for (int m = 0; m < 4; ++m) { const bf16x8 qf = pack8(acc[0][m], acc[1][m]); pp[m][0] = rbf4(MFMA(wp[0], qf, zero)); pp[m][1] = rbf4(MFMA(wp[1], qf, zero)); }
    dual_max(pp, mx, nm);
  }
  const u32x4 onesu = {0x3f803f80u, 0x3f803f80u, 0x3f803f80u, 0x3f803f80u};
  const bf16x8 ones = __builtin_bit_cast(bf16x8, onesu);
  const auto rkv = __builtin_amdgcn_make_buffer_rsrc(ws + KVP_OFF, 0, H_ * 8 * KVP_STRIDE, 0x00020000);
  const int t2 = tid & 255;
  g2_setup_a(d, ws, wid, lane);
  asm volatile("" : "+v"(mx[0]), "+v"(mx[1]), "+v"(mx[2]), "+v"(mx[3]), "+v"(nm[0]), "+v"(nm[1]), "+v"(nm[2]), "+v"(nm[3]));
  wait_vm<0>();
  bar();
  if (tid < 4) post(fh + ((tid * 2 + (rb >> 2)) * 8 + rb) * 32 + xc * 4 + tl, tag);
  if constexpr (KV) {
    if (wid >= 4) {
      const int pc = ((rb * 2 + tl) * 8 + xc) * 4 + wm;
      if (pc < 192) touch(make_srd(w2, F_ * 2048u), lane * 128, ((pc & 3) * 28 + 22) * 65536 + (pc >> 2) * 8192, G1_SCR);
      else touch(make_srd(wo, 1024u * 2048u), lane * 128, (pc - 192) * 8192, G1_SCR);
    }
#pragma unroll
    for (int m = 0; m < 4; ++m) { pos[m] = half_exp(pp[m], L2E, mx[m]); neg[m] = half_exp(pp[m], -L2E, nm[m]); }
    bf16x8 idm[2];
#pragma unroll
    for (int ft = 0; ft < 2; ++ft) {
      const unsigned one = (g == q) ? ((p & 1) ? 0x3f800000u : 0x00003f80u) : 0u;
      u32x4 iv = {0u, 0u, 0u, 0u};
      iv[ft * 2] = (p >> 1) == 0 ? one : 0u; iv[ft * 2 + 1] = (p >> 1) == 1 ? one : 0u;
      idm[ft] = __builtin_bit_cast(bf16x8, iv);
    }
    f32x4 kv[4][2], zz[4];
#pragma unroll
    for (int f4 = 0; f4 < 4; ++f4) { kv[f4][0] = zero; kv[f4][1] = zero; zz[f4] = zero; }
#pragma unroll
    for (int u = 0; u < 2; ++u) {
      f32x4 tr[2][4];
#pragma unroll
      for (int mm = 0; mm < 2; ++mm) {
        const int m = 2 * u + mm;
        const f32x4 sp = MFMA(pos[m], ones, zero), sn = MFMA(neg[m], ones, zero);
        f32x4 rp, rn;
#pragma unroll
        for (int r = 0; r < 4; ++r) { rp[r] = __builtin_amdgcn_rcpf(sp[r]); rn[r] = __builtin_amdgcn_rcpf(sn[r]); }
        tr[mm][0] = MFMA(pos[m], idm[0], zero) * rp; tr[mm][1] = MFMA(pos[m], idm[1], zero) * rp;
        tr[mm][2] = MFMA(neg[m], idm[0], zero) * rn; tr[mm][3] = MFMA(neg[m], idm[1], zero) * rn;
      }
      const bf16x8 v0 = pack8(acc[2][2 * u], acc[2][2 * u + 1]), v1 = pack8(acc[3][2 * u], acc[3][2 * u + 1]);
#pragma unroll
      for (int f4 = 0; f4 < 4; ++f4) {
        const bf16x8 ph = pack8(tr[0][f4], tr[1][f4]);
        kv[f4][0] = MFMA(ph, v0, kv[f4][0]); kv[f4][1] = MFMA(ph, v1, kv[f4][1]); zz[f4] = MFMA(ph, ones, zz[f4]);
      }
    }
    const unsigned red = wm * G2_SLOT + grp * 8192, redz = KVS + wid * 256;
#pragma unroll
    for (int f4 = 0; f4 < 4; ++f4) {
#pragma unroll
      for (int t = 0; t < 2; ++t) *(LAS f32x4*)(red + (f4 & 1) * 4096 + ((f4 >> 1) * 2 + t) * 1024 + lane * 16) = kv[f4][t];
      if ((lane & 15) == 0) *(LAS f32x4*)(redz + ((f4 >> 1) * 4 + g) * 32 + (f4 & 1) * 16) = zz[f4];
    }
    wait_lgkm0();
    bar();
    {
      f32x4 s0 = zero, s1 = zero;
#pragma unroll
      for (int w = 0; w < 4; ++w) { s0 += *(const LAS f32x4*)(w * G2_SLOT + grp * 8192 + t2 * 16); s1 += *(const LAS f32x4*)(w * G2_SLOT + grp * 8192 + 4096 + t2 * 16); }
      __builtin_amdgcn_raw_buffer_store_b128(__builtin_bit_cast(u32x4, pack8(s0, s1)), rkv, (hd * 8 + rb) * KVP_STRIDE + t2 * 16, 0, SC1);
      if (t2 >= 248) {
        s0 = zero; s1 = zero;
#pragma unroll
        for (int w = 0; w < 4; ++w) { s0 += *(const LAS f32x4*)(KVS + (grp * 4 + w) * 256 + (t2 - 248) * 32); s1 += *(const LAS f32x4*)(KVS + (grp * 4 + w) * 256 + (t2 - 248) * 32 + 16); }
        __builtin_amdgcn_raw_buffer_store_b128(__builtin_bit_cast(u32x4, pack8(s0, s1)), rkv, (hd * 8 + rb) * KVP_STRIDE + 4096 + (t2 - 248) * 16, 0, SC1);
      }
    }
    wait_vm<0>(); wait_lgkm0();
    if (wid == 1) await_all(fh + (xc * 8 + g2_pos().rb) * 32 + (xc >> 1) * 8 + (lane & 7), tag);
    bar();
    gu64* fk = flg + (FL_KV + hp) * 32;
    if (tid == 0) post(fk + rb, tag);
    g2_first_a<0>(d); g2_first_a<1>(d); g2_first_a<2>(d); g2_first_a<3>(d); g2_first_a<4>(d); g2_first_a<5>(d);
#pragma unroll
    for (int m = 0; m < 4; ++m) qs.eps[m] = 0.f;
  } else {
#pragma unroll
    for (int m = 0; m < 2; ++m) { q_tile(pp[m], mx[m], nm[m], pos[m], neg[m], qs.eps[m]); asm volatile("" : "+v"(pos[m]), "+v"(neg[m]), "+v"(qs.eps[m])); }
    if (wid == 1) await_all(fh + (xc * 8 + g2_pos().rb) * 32 + (xc >> 1) * 8 + (lane & 7), tag);
    bar();
    g2_first_a<0>(d); g2_first_a<1>(d);
    SB(); q_tile(pp[2], mx[2], nm[2], pos[2], neg[2], qs.eps[2]); SB();
    g2_first_a<2>(d); g2_first_a<3>(d);
    SB(); q_tile(pp[3], mx[3], nm[3], pos[3], neg[3], qs.eps[3]); SB();
    g2_first_a<4>(d); g2_first_a<5>(d);
    asm volatile("" : "+v"(pos[2]), "+v"(neg[2]), "+v"(qs.eps[2]), "+v"(pos[3]), "+v"(neg[3]), "+v"(qs.eps[3]));
  }
}


DI void o_stage(char* __restrict__ ws, const QState& qs, int rb, int hp, int wid, int lane) {
  const int wm = wid & 3, grp = wid >> 2, g = lane >> 4, hd = hp * 2 + grp;
  const f32x4 zero = {0.f, 0.f, 0.f, 0.f};
  const unsigned ks = KVS + grp * 4352;
  bf16x8 kv[2][2], zf[2];
#pragma unroll
  for (int pn = 0; pn < 2; ++pn) {
    kv[pn][0] = lds_b128(ks + (pn * 2) * 1024 + lane * 16); kv[pn][1] = lds_b128(ks + (pn * 2 + 1) * 1024 + lane * 16);
    zf[pn] = lds_b128(ks + 4096 + (pn * 4 + g) * 16);
  }
  const auto ra2 = __builtin_amdgcn_make_buffer_rsrc(ws + A2_OFF, 0, (unsigned)A2_BYTES, 0x00020000);
  const unsigned ob = ((rb * KS2 + 112 + hd) * 16 + wm * 4) * 1024 + lane * 16;
#pragma unroll
  for (int m = 0; m < 4; ++m) {
    const f32x4 n0 = MFMA(kv[1][0], qs.neg[m], MFMA(kv[0][0], qs.pos[m], zero)), n1 = MFMA(kv[1][1], qs.neg[m], MFMA(kv[0][1], qs.pos[m], zero));
    const float inv = __builtin_amdgcn_rcpf(MFMA(zf[1], qs.neg[m], MFMA(zf[0], qs.pos[m], zero))[0] + qs.eps[m]);
    __builtin_amdgcn_raw_buffer_store_b128(__builtin_bit_cast(u32x4, pack8(n0 * inv, n1 * inv)), ra2, ob + m * 1024, 0, SC1);
  }
  wait_lgkm0();
}
DI void peek_row(char* ws, int row, int wid, int lane) {
  touch_dev(make_srd(ws + FLG_OFF + row * 256, 256), lane * 4, 0, OFL + wid * 256);
}
template <int VM> DI void check_row(char* ws, int row, int word, u64 tag, int wid) {
  wait_vm<VM>();
  if (!__all(*(const LAS u64*)(OFL + wid * 256 + word * 8) == tag)) await_all((gu64*)(ws + FLG_OFF) + row * 32 + word, tag);
}
constexpr int g2_na(int s) { return s + LA <= 36 ? 2 : 0; }
constexpr int g2_nb(int s) { return s >= 0 && s <= 30 ? 1 : 0; }
constexpr int g2_vm(int s) { int n = g2_nb(s + 2 - LA); for (int j = s + 3 - LA; j <= s; ++j) n += g2_na(j) + g2_nb(j); return n; }
template <int S, int J> DI void g2_memop(Frag<4>& n, const Adr& ad, Dma& d) {
  constexpr int ND = g2_na(S) + g2_nb(S), TOT = 12 + ND, before = (J * ND) / TOT, after = ((J + 1) * ND) / TOT;
  if constexpr (after > before) k_dma<g2_nb(S), (S % 6) * G2_SLOT, before>(d);
  else if constexpr (S < 35 && (J - before != 2 || S + 1 < G2_LATE)) k_read<4, 2048, (((S + 1) % 6) & 1) * G2_SLOT, J - before>(n, ad);
  if constexpr (J == 0 && ND > 0) set_m0<((S + LA) % 6) * G2_SLOT>(d.lda);
}
template <int S, int I, int... J> DI void g2_gap(f32x4 (&acc)[4][4], const Frag<4>& c, Frag<4>& n, const Adr& ad, Dma& d, std::integer_sequence<int, J...>) {
  constexpr int MT = S < G2_LATE ? 4 : 3, NM = 4 * MT, TOT = sizeof...(J), SPAN = NM - KTAIL;
  constexpr int t = I / MT, m = 4 - MT + ((t & 1) ? MT - 1 - I % MT : I % MT);
  if constexpr (S == 0) mfma_new(acc[t][m], cat(c.b[t][0], c.b[t][1]), c.a[m]); else mfma_acc(acc[t][m], cat(c.b[t][0], c.b[t][1]), c.a[m]);
  SB();
  if constexpr (I == NM - 1) __builtin_amdgcn_s_setprio(3);
  if constexpr (I == SPAN / 3) __builtin_amdgcn_s_setprio(2);
  if constexpr (I == 2 * SPAN / 3) __builtin_amdgcn_s_setprio(1);
  if constexpr (I == SPAN) __builtin_amdgcn_s_setprio(0);
  (((J * SPAN / TOT == I) ? g2_memop<S, J>(n, ad, d) : (void)0), ...);
  if constexpr (I == NM - 2) { wait_lgkm0(); if constexpr (S < 34) wait_vm<g2_vm(S)>(); }
  SB();
}
template <int S, int... I> DI void g2_gaps(f32x4 (&acc)[4][4], const Frag<4>& c, Frag<4>& n, const Adr& ad, Dma& d, std::integer_sequence<int, I...>) {
  (g2_gap<S, I>(acc, c, n, ad, d, std::make_integer_sequence<int, 12 + g2_na(S) + g2_nb(S)>{}), ...);
}
template <class F, int... S> DI void for_each_index(F& f, std::integer_sequence<int, S...>) { (f(std::integral_constant<int, S>{}), ...); }
DI void g2_body(char* __restrict__ ws, const u16* __restrict__ wo, const u16* __restrict__ b2, u16* __restrict__ out, u64 tag, Dma& d, const QState& qs, bool isq, int my_o, int wid) {
  int lane = fresh_lane();
  asm volatile("" : "+v"(lane));
  const int tid = wid * 64 + lane;
  const int wm = wid & 3, wn = wid >> 2;
  const G2Pos ps = g2_pos();
  const int kq = ps.kq, rb = ps.rb, ct = ps.ct;
  const int g = lane >> 4, q = (lane & 15) >> 2, p = lane & 3;
  gu64* flg = (gu64*)(ws + FLG_OFF);
  Adr ad0;
  ad0.a = wm * 4096 + lane * 16;
  ad0.l0 = 16384 + wn * 4096 + (g >> 1) * 1024 + (g & 1) * 512 + q * 64 + (g & 1) * 32 + p * 8; ad0.l1 = ad0.l0 ^ 32; ad0.lx = 0;
  ad0 = adr_at(ad0, 0);
  const int G = ((g & 1) << 1) | (g >> 1);
  const unsigned go = (rb * 256 + wm * 64 + kq * 16 + (lane & 15)) * D_ + ct * 128 + wn * 64 + G * 8;
  f32x4 acc[4][4];
  const Adr ad1 = adr_at(ad0, 2 * G2_SLOT), ad2 = adr_at(ad0, 4 * G2_SLOT);
  {
    Frag<4> f0, f1;
    wait_vm<2 * LA - 2>();
    bar();
    k_read_all<4, 2048, 0>(f0, ad0, std::make_integer_sequence<int, 12>{});
    wait_lgkm0();
    wait_vm<2 * LA - 4>();
    const bool hw = wid == 3 || wid == 4;
    const int o_hp = my_o & 31, o_rb = my_o >> 5;
    constexpr int OS = O_STEP + 2 * HOP + 1;
    auto step = [&](auto sc) {
      constexpr int S = decltype(sc)::value, rs = (S + 1) % 6;
      bar(); SB();
      if constexpr (S == SH_AT) { if (!isq && hw) peek_row(ws, FL_KV + o_hp, wid, lane); }
      if constexpr (S == SH_AT + HOP) {
        if (!isq && hw) {
          check_row<3 * HOP>(ws, FL_KV + o_hp, lane & 7, tag, wid);
          const i32x4 sp = make_srd(ws + KVP_OFF + (o_hp * 2 + wn) * 8 * KVP_STRIDE, 8 * KVP_STRIDE);
          const unsigned vo = (lane >> 5) * KVP_STRIDE + (o_rb * 32 + (lane & 31)) * 16;
#pragma unroll
          for (int j = 0; j < 4; ++j) { set_m0<KVS>(wn * 4352 + j * 1024); nop1(); dma_ld_dev<0>(sp, vo, j * 2 * KVP_STRIDE); }
          touch_dev(sp, lane < 32 ? (lane >> 2) * KVP_STRIDE + 4096u + o_rb * 16 + (lane & 3) * 4 : OOB, 0, KVS + wn * 4352 + 4096);
        }
      }
      if constexpr (S == SH_AT + 2 * HOP) {
        if (!isq && hw) {
          wait_vm<3 * HOP>();
          f32x4 sv = {0.f, 0.f, 0.f, 0.f};
          f32x2 sz = {0.f, 0.f};
#pragma unroll
          for (int b = 0; b < 8; ++b) {
            const u32x2 v = *(const LAS u32x2*)(KVS + wn * 4352 + b * 512 + lane * 8);
            const unsigned z = *(const LAS unsigned*)(KVS + wn * 4352 + 4096 + b * 16 + (lane & 3) * 4);
            sv += (f32x4){lo_f(v[0]), hi_f(v[0]), lo_f(v[1]), hi_f(v[1])}; sz += (f32x2){lo_f(z), hi_f(z)};
          }
          const auto rt = __builtin_amdgcn_make_buffer_rsrc(ws + KVT_OFF, 0, H_ * KVP_STRIDE, 0x00020000);
          __builtin_amdgcn_raw_buffer_store_b64((u32x2){cvtpk(sv[0], sv[1]), cvtpk(sv[2], sv[3])}, rt, o_rb * 512 + lane * 8, (o_hp * 2 + wn) * KVP_STRIDE, SC1);
          __builtin_amdgcn_raw_buffer_store_b32(cvtpk(sz[0], sz[1]), rt, lane < 4 ? 4096u + o_rb * 16 + lane * 4 : OOB, (o_hp * 2 + wn) * KVP_STRIDE, SC1);
        }
      }
      if constexpr (S == SH_AT + 3 * HOP) { if (!isq && hw) { wait_vm<0>(); if (lane == 0) post(flg + (FL_KT + o_hp) * 32 + wn * 8 + o_rb, tag); } }
      if constexpr (S == O_STEP) { if (isq && hw) peek_row(ws, FL_KT + o_hp, wid, lane); }
      if constexpr (S == O_STEP + HOP) {
        if (isq && hw) {
          check_row<3 * HOP>(ws, FL_KT + o_hp, wn * 8 + (lane & 7), tag, wid);
          const i32x4 st = make_srd(ws + KVT_OFF + (o_hp * 2 + wn) * KVP_STRIDE, KVP_STRIDE);
          set_m0<KVS>(wn * 4352); nop1(); dma_ld_dev<0>(st, lane * 16, 0); dma_ld_dev<1024>(st, lane * 16, 0); dma_ld_dev<2048>(st, lane * 16, 0); dma_ld_dev<3072>(st, lane * 16, 0);
          touch_dev(st, 4096 + lane * 4, 0, KVS + wn * 4352 + 4096);
        }
      }
      if constexpr (S == O_STEP + 2 * HOP) { if (hw) wait_vm<3 * HOP>(); }
      if constexpr (S == OS) { if (isq) o_stage(ws, qs, o_rb, o_hp, wid, lane); }
      if constexpr (S == OS + 2 || S == OS + 3) { if (isq && wn == S - OS - 2) wait_vm<0>(); }
      if constexpr (S == OS + 4) { if (isq && tid == 0) post(flg + (FL_O + o_rb * 4 + (o_hp >> 2)) * 32 + (o_hp & 3), tag); }
      if constexpr (S == 33 - LA) { if (wid == 2) peek_row(ws, FL_H + (blockIdx.x & 7) * 8 + rb, wid, lane); }
      if constexpr (S == 35 - LA) { if (wid == 2) check_row<g2_na(33 - LA) + g2_nb(33 - LA) + g2_na(34 - LA) + g2_nb(34 - LA)>(ws, FL_H + (blockIdx.x & 7) * 8 + rb, lane & 31, tag, wid); }
      if constexpr (S == 25 - LA) { if (wid == 2) peek_row(ws, FL_O + rb * 4 + kq, wid, lane); }
      if constexpr (S == 27 - LA) { if (wid == 2) check_row<6>(ws, FL_O + rb * 4 + kq, lane & 3, tag, wid); }
      if constexpr (S == 28 - LA) d.soa = ((rb * KS2 + 112 + kq * 8) * 16 + (wid >> 1) * 4) * 1024;
      if constexpr (S == 22) { d.sb = make_srd(wo, 1024u * 2048u); d.sob = g2_sob0(wid, ct) + kq * 256 * 2048; }
      if constexpr (S == 36 - LA) { d.sa = make_srd(ws + XR_OFF, S_ * D_ * 2); d.va0 = lane * 16; d.va1 = lane * 16 + 131072 - 1024; d.soa = ((ct * 4 + wn * 2) * 128 + rb * 16 + wm * 4 + kq) * 1024; }
      if constexpr (S == 30) { d.sb = make_srd(b2 + ct * 128, 256); d.vb0 = lane * 16; d.sob = 0; }
      if constexpr (g2_natural(S + 6) != g2_natural(S + 5) && S > 0) { const unsigned t_ = d.vb0; d.vb0 = d.vb1; d.vb1 = t_; }
      SB();
      g2_gaps<S>(acc, (S & 1) ? f1 : f0, (S & 1) ? f0 : f1, rs < 2 ? ad0 : rs < 4 ? ad1 : ad2, d, std::make_integer_sequence<int, (S < G2_LATE ? 16 : 12)>{});
    };
    for_each_index(step, std::make_integer_sequence<int, 36>{});
    __builtin_amdgcn_s_setprio(0);
    acc_fence();
  }
  const auto rx = __builtin_amdgcn_make_buffer_rsrc(ws + XCH_OFF, 0, 256u * 49152u, 0x00020000);
#pragma unroll
  for (int i = 1; i < 4; ++i)
#pragma unroll
    for (int h = 0; h < 2; ++h)
      __builtin_amdgcn_raw_buffer_store_b128(__builtin_bit_cast(u32x4, pack8(acc[2 * h][i], acc[2 * h + 1][i])), rx, ((blockIdx.x & ~6) | (((kq + i) & 3) << 1)) * 49152 + (((i - 1) * 2 + h) * 8 + wid) * 1024 + lane * 16, 0, SC1);
  SB();
#pragma unroll
  for (int st = G2_LATE; st < 36; ++st) {
    const Adr& ad = st % 6 < 2 ? ad0 : st % 6 < 4 ? ad1 : ad2;
    const int ro = (st % 6 & 1) * G2_SLOT;
    const bf16x8 a0 = lds_b128(ad.a + ro);
#pragma unroll
    for (int t = 0; t < 4; ++t) { const unsigned l = ((t & 1) ? ad.l1 : ad.l0) + ro + (t >> 1) * 2048; acc[t][0] = MFMA(cat(lds_tr(l), lds_tr(l + 256)), a0, acc[t][0]); }
  }
  wait_vm<6>();
#pragma unroll
  for (int t = 0; t < 4; ++t) {
    acc[t][0] += lds_bias(d.lda + (t >> 1) * 1024 + (((t & 1) * 2 + (g >> 1)) * 16 + (lane & 15)) * 16 + (g & 1) * 8) + lds_bias(d.ldb + (wn * 64 + t * 16 + g * 4) * 2);
    asm volatile("" : "+v"(acc[t][0]));
  }
  wait_vm<0>();
  if (lane < 3) post(flg + FL_X * 32 + ((((blockIdx.x & ~6) | (((kq + 1 + lane) & 3) << 1)) * 8 + wid) * 4 + lane) * 4, tag);
  __builtin_amdgcn_s_sleep(16);
  await_all(flg + FL_X * 32 + ((blockIdx.x * 8 + wid) * 4 + lane % 3) * 4, tag);
  __builtin_amdgcn_fence(__ATOMIC_ACQUIRE, "wavefront");
  const auto rout = __builtin_amdgcn_make_buffer_rsrc(out, 0, S_ * D_ * 2, 0x00020000);
  u32x4 pr[3][2];
#pragma unroll
  for (int h = 0; h < 2; ++h)
#pragma unroll
    for (int j = 0; j < 3; ++j) pr[j][h] = __builtin_amdgcn_raw_buffer_load_b128(rx, blockIdx.x * 49152 + ((j * 2 + h) * 8 + wid) * 1024 + lane * 16, 0, SC1);
  u32x4 ov[2];
#pragma unroll
  for (int h = 0; h < 2; ++h) {
    f32x4 v0 = acc[2 * h][0], v1 = acc[2 * h + 1][0];
#pragma unroll
    for (int j = 0; j < 3; ++j) {
      v0 += (f32x4){lo_f(pr[j][h][0]), hi_f(pr[j][h][0]), lo_f(pr[j][h][1]), hi_f(pr[j][h][1])};
      v1 += (f32x4){lo_f(pr[j][h][2]), hi_f(pr[j][h][2]), lo_f(pr[j][h][3]), hi_f(pr[j][h][3])};
    }
    ov[h] = frag_natural(cvtpk(v0[0], v0[1]), cvtpk(v0[2], v0[3]), cvtpk(v1[0], v1[1]), cvtpk(v1[2], v1[3]));
  }
#pragma unroll
  for (int h = 0; h < 2; ++h) __builtin_amdgcn_raw_buffer_store_b128(ov[h], rout, (unsigned)(go + h * 32) * 2, 0, SC1);
  if (blockIdx.x == 0 && tid == 0) post(flg, tag);
}

__global__ __launch_bounds__(512, 2) void k_all(char* __restrict__ ws, const u16* __restrict__ x, const u16* __restrict__ wqkv, const u16* __restrict__ wphi, const u16* __restrict__ wo,
                                                const u16* __restrict__ w1, const u16* __restrict__ b1, const u16* __restrict__ w2, const u16* __restrict__ b2, u16* __restrict__ out) {
  u64 pc0;
  asm volatile("s_getpc_b64 %0" : "=s"(pc0));
  const int xc = blockIdx.x & 7, idx = blockIdx.x >> 3, rb = idx >> 2, tl = idx & 3;
  XKeep xk; Dma d; QState qs;
  const u64 tag = start_phase(ws, x, wqkv, wphi, w1, b1, b2, rb, xc, tl, xk, d, pc0);
  if (tl < 2) g1_body<true>(ws, wqkv, w1, w2, wo, rb, xc, tl, tag, d, qs, xk);
  else g1_body<false>(ws, wqkv, w1, w2, wo, rb, xc, tl, tag, d, qs, xk);
  g2_body(ws, wo, b2, out, tag, d, qs, tl >= 2, rb * 32 + head_pair(xc, tl), __builtin_amdgcn_readfirstlane(threadIdx.x >> 6));
}

extern "C" void kernel_launch(void* const* d_in, const int* in_sizes, int n_in, void* d_out, int out_size, void* d_ws, size_t ws_size, hipStream_t stream) {
  const u16* x = (const u16*)d_in[0]; const u16* wqkv = (const u16*)d_in[1]; const u16* wphi = (const u16*)d_in[2]; const u16* wo = (const u16*)d_in[3];
  const u16* w1 = (const u16*)d_in[5]; const u16* b1 = (const u16*)d_in[6]; const u16* w2 = (const u16*)d_in[7]; const u16* b2 = (const u16*)d_in[8];
  char* ws = (char*)d_ws;
  constexpr int LDS = LDS_BYTES;
  (void)hipFuncSetAttribute((const void*)k_all, hipFuncAttributeMaxDynamicSharedMemorySize, LDS);
  k_all<<<256, 512, LDS, stream>>>(ws, x, wqkv, wphi, wo, w1, b1, w2, b2, (u16*)d_out);
}
```
